```python
import torch

def _softmax(x, axis=-1):
    return torch.softmax(x, dim=axis)
def _gelu(x):
    return torch.nn.functional.gelu(x, approximate="tanh")
def _silu(x):
    return torch.nn.functional.silu(x)
def _sigmoid(x):
    return torch.sigmoid(x)
def _layernorm(x, axis=-1, eps=1e-5):
    m = x.mean(dim=axis, keepdim=True)
    v = x.var(dim=axis, keepdim=True, unbiased=False)
    return (x - m) / torch.sqrt(v + eps)
def _scatter_add(tgt, idx, src, axis=0):
    return tgt.clone().index_add_(axis, idx, src)
def _segment_sum(src, seg, n):
    out = torch.zeros((n,) + src.shape[1:], dtype=src.dtype)
    return out.index_add_(0, seg, src)
def _pad(x, pw):
    flat = tuple(v for pair in reversed(pw) for v in pair)
    return torch.nn.functional.pad(x, flat)

kernel_name = 'standalone__mixer_token_mlp__sampling'

def setup_inputs():

    _w = (lambda a: (lambda t: t.to(torch.bfloat16) if t.is_floating_point() else t)(torch.as_tensor(a)))
    B, S, D, F = 32, 32, 576, 4608
    return {
        "x": _w((lambda *s: torch.randn(*s, dtype=torch.bfloat16) * 0.05)(B, S, D)),
        "w1": _w((lambda *s: torch.randn(*s, dtype=torch.bfloat16) * 0.2)(S, F) * (S ** -0.5)),
        "b1": _w((lambda *s: torch.randn(*s, dtype=torch.bfloat16) * 3.2)(F) * 0.02),
        "w2": _w((lambda *s: torch.randn(*s, dtype=torch.bfloat16) * 0.05)(F, S) * (F ** -0.5)),
        "b2": _w((lambda *s: torch.randn(*s, dtype=torch.bfloat16) * 0.1)(S) * 0.02),
    }

def reference(x, w1, b1, w2, b2):
    B, S, D = x.shape
    h = _layernorm(x, axis=-1)
    u = torch.einsum("bsd,sf->bdf", h, w1) + b1
    u = _gelu(u)
    y = torch.einsum("bdf,fs->bds", u, w2) + b2
    return (lambda x, *d: x.permute(*d))(y, 0, 2, 1)
```

```cpp
#include <hip/hip_runtime.h>
#include <hip/hip_bf16.h>
namespace {
constexpr int B=32,S=32,D=576,F=4608;
__device__ float bf(unsigned short v){ return __uint_as_float((unsigned)v<<16); }
__device__ unsigned short tobf(float f){ unsigned u=__float_as_uint(f); return (unsigned short)((u+0x7fff+((u>>16)&1))>>16); }
__global__ void naive(const unsigned short* x,const unsigned short* w1,const unsigned short* b1,const unsigned short* w2,const unsigned short* b2,unsigned short* out){
  __shared__ float h[S]; __shared__ float mean[S], rstd[S]; __shared__ float g[F];
  int b=blockIdx.x/D, d=blockIdx.x%D, t=threadIdx.x;
  if(t<S){ float s=0; for(int i=0;i<D;i++) s+=bf(x[(b*S+t)*D+i]); float m=s/D; float v=0; for(int i=0;i<D;i++){float e=bf(x[(b*S+t)*D+i])-m; v+=e*e;} v/=D; h[t]=bf(tobf((bf(x[(b*S+t)*D+d])-m)*rsqrtf(v+1e-5f))); }
  __syncthreads();
  for(int f=t;f<F;f+=256){ float u=0; for(int s=0;s<S;s++) u+=h[s]*bf(w1[s*F+f]); u+=bf(b1[f]); float z=0.7978845608f*(u+0.044715f*u*u*u); g[f]=bf(tobf(0.5f*u*(1.f+tanhf(z)))); }
  __syncthreads();
  if(t<S){ float y=0; for(int f=0;f<F;f++) y+=g[f]*bf(w2[f*S+t]); y+=bf(b2[t]); out[(b*S+t)*D+d]=tobf(y); }
}
}
extern "C" void kernel_launch(void* const* d_in, const int* in_sizes, int n_in, void* d_out, int out_size, void* d_ws, size_t ws_size, hipStream_t stream) {
  naive<<<B*D,256,0,stream>>>((const unsigned short*)d_in[0],(const unsigned short*)d_in[1],(const unsigned short*)d_in[2],(const unsigned short*)d_in[3],(const unsigned short*)d_in[4],(unsigned short*)d_out);
}
```

```cpp
#include <hip/hip_runtime.h>
namespace {
struct MxArgs { const void* x; const void* w1; const void* b1; const void* w2; const void* b2; void* out; void* ws; };
__device__ const unsigned kGeluTab[4096] __attribute__((aligned(256))) = {
0x33733371,0x33773375,0x337a3378,0x337e337c,0x33813380,0x33833382,0x33853384,0x33873386,0x33883387,0x338a3389,0x338c338b,0x338e338d,0x3390338f,0x33923391,0x33943393,0x33963395,
0x33973396,0x33993398,0x339b339a,0x339d339c,0x339f339e,0x33a133a0,0x33a333a2,0x33a533a4,0x33a633a5,0x33a833a7,0x33aa33a9,0x33ac33ab,0x33ae33ad,0x33b033af,0x33b233b1,0x33b433b3,
0x33b533b4,0x33b733b6,0x33b933b8,0x33bb33ba,0x33bd33bc,0x33bf33be,0x33c133c0,0x33c233c2,0x33c433c3,0x33c633c5,0x33c833c7,0x33ca33c9,0x33cc33cb,0x33ce33cd,0x33d033cf,0x33d133d1,
0x33d333d2,0x33d533d4,0x33d733d6,0x33d933d8,0x33db33da,0x33dd33dc,0x33df33de,0x33e033e0,0x33e233e1,0x33e433e3,0x33e633e5,0x33e833e7,0x33ea33e9,0x33ec33eb,0x33ee33ed,0x33ef33ef,
0x33f333f1,0x33f733f5,0x33fa33f8,0x33fe33fc,0x34013400,0x34033402,0x34053404,0x34073406,0x34083407,0x340a3409,0x340c340b,0x340e340d,0x3410340f,0x34123411,0x34143413,0x34163415,
0x34173416,0x34193418,0x341b341a,0x341d341c,0x341f341e,0x34213420,0x34233422,0x34253424,0x34263425,0x34283427,0x342a3429,0x342c342b,0x342e342d,0x3430342f,0x34323431,0x34343433,
0x34353434,0x34373436,0x34393438,0x343b343a,0x343d343c,0x343f343e,0x34413440,0x34423442,0x34443443,0x34463445,0x34483447,0x344a3449,0x344c344b,0x344e344d,0x3450344f,0x34513451,
0x34533452,0x34553454,0x34573456,0x34593458,0x345b345a,0x345d345c,0x345f345e,0x34603460,0x34623461,0x34643463,0x34663465,0x34683467,0x346a3469,0x346c346b,0x346e346d,0x346f346f,
0x34733471,0x34773475,0x347a3478,0x347e347c,0x34813480,0x34833482,0x34853484,0x34873486,0x34883487,0x348a3489,0x348c348b,0x348e348d,0x3490348f,0x34923491,0x34943493,0x34963495,
0x34973496,0x34993498,0x349b349a,0x349d349c,0x349f349e,0x34a134a0,0x34a334a2,0x34a534a4,0x34a634a5,0x34a834a7,0x34aa34a9,0x34ac34ab,0x34ae34ad,0x34b034af,0x34b234b1,0x34b434b3,
0x34b534b4,0x34b734b6,0x34b934b8,0x34bb34ba,0x34bd34bc,0x34bf34be,0x34c134c0,0x34c234c2,0x34c434c3,0x34c634c5,0x34c834c7,0x34ca34c9,0x34cc34cb,0x34ce34cd,0x34d034cf,0x34d134d1,
0x34d334d2,0x34d534d4,0x34d734d6,0x34d934d8,0x34db34da,0x34dd34dc,0x34df34de,0x34e034e0,0x34e234e1,0x34e434e3,0x34e634e5,0x34e834e7,0x34ea34e9,0x34ec34eb,0x34ee34ed,0x34ef34ef,
0x34f334f1,0x34f734f5,0x34fa34f8,0x34fe34fc,0x35013500,0x35033502,0x35053504,0x35073506,0x35083507,0x350a3509,0x350c350b,0x350e350d,0x3510350f,0x35123511,0x35143513,0x35163515,
0x35173516,0x35193518,0x351b351a,0x351d351c,0x351f351e,0x35213520,0x35233522,0x35253524,0x35263525,0x35283527,0x352a3529,0x352c352b,0x352e352d,0x3530352f,0x35323531,0x35343533,
0x35353534,0x35373536,0x35393538,0x353b353a,0x353d353c,0x353f353e,0x35413540,0x35423542,0x35443543,0x35463545,0x35483547,0x354a3549,0x354c354b,0x354e354d,0x3550354f,0x35513551,
0x35533552,0x35553554,0x35573556,0x35593558,0x355b355a,0x355d355c,0x355f355e,0x35603560,0x35623561,0x35643563,0x35663565,0x35683567,0x356a3569,0x356c356b,0x356e356d,0x356f356f,
0x35733571,0x35773575,0x357a3578,0x357e357c,0x35813580,0x35833582,0x35853584,0x35873586,0x35883587,0x358a3589,0x358c358b,0x358e358d,0x3590358f,0x35923591,0x35943593,0x35963595,
0x35973596,0x35993598,0x359b359a,0x359d359c,0x359f359e,0x35a135a0,0x35a335a2,0x35a535a4,0x35a635a5,0x35a835a7,0x35aa35a9,0x35ac35ab,0x35ae35ad,0x35b035af,0x35b235b1,0x35b435b3,
0x35b535b4,0x35b735b6,0x35b935b8,0x35bb35ba,0x35bd35bc,0x35bf35be,0x35c135c0,0x35c235c2,0x35c435c3,0x35c635c5,0x35c835c7,0x35ca35c9,0x35cc35cb,0x35ce35cd,0x35d035cf,0x35d135d1,
0x35d335d2,0x35d535d4,0x35d735d6,0x35d935d8,0x35db35da,0x35dd35dc,0x35df35de,0x35e035e0,0x35e235e1,0x35e435e3,0x35e635e5,0x35e835e7,0x35ea35e9,0x35ec35eb,0x35ee35ed,0x35ef35ef,
0x35f335f1,0x35f735f5,0x35fa35f8,0x35fe35fc,0x36013600,0x36033602,0x36053604,0x36073606,0x36083607,0x360a3609,0x360c360b,0x360e360d,0x3610360f,0x36123611,0x36143613,0x36163615,
0x36173616,0x36193618,0x361b361a,0x361d361c,0x361f361e,0x36213620,0x36233622,0x36253624,0x36263625,0x36283627,0x362a3629,0x362c362b,0x362e362d,0x3630362f,0x36323631,0x36343633,
0x36353634,0x36373636,0x36393638,0x363b363a,0x363d363c,0x363f363e,0x36413640,0x36423642,0x36443643,0x36463645,0x36483647,0x364a3649,0x364c364b,0x364e364d,0x3650364f,0x36513651,
0x36533652,0x36553654,0x36573656,0x36593658,0x365b365a,0x365d365c,0x365f365e,0x36603660,0x36623661,0x36643663,0x36663665,0x36683667,0x366a3669,0x366c366b,0x366e366d,0x366f366f,
0x36733671,0x36773675,0x367a3678,0x367e367c,0x36813680,0x36833682,0x36853684,0x36873686,0x36883687,0x368a3689,0x368c368b,0x368e368d,0x3690368f,0x36923691,0x36943693,0x36963695,
0x36973696,0x36993698,0x369b369a,0x369d369c,0x369f369e,0x36a136a0,0x36a336a2,0x36a536a4,0x36a636a5,0x36a836a7,0x36aa36a9,0x36ac36ab,0x36ae36ad,0x36b036af,0x36b236b1,0x36b436b3,
0x36b536b4,0x36b736b6,0x36b936b8,0x36bb36ba,0x36bd36bc,0x36bf36be,0x36c136c0,0x36c236c2,0x36c436c3,0x36c636c5,0x36c836c7,0x36ca36c9,0x36cc36cb,0x36ce36cd,0x36d036cf,0x36d136d1,
0x36d336d2,0x36d536d4,0x36d736d6,0x36d936d8,0x36db36da,0x36dd36dc,0x36df36de,0x36e036e0,0x36e236e1,0x36e436e3,0x36e636e5,0x36e836e7,0x36ea36e9,0x36ec36eb,0x36ee36ed,0x36ef36ef,
0x36f336f1,0x36f736f5,0x36fa36f8,0x36fe36fc,0x37013700,0x37033702,0x37053704,0x37073706,0x37083707,0x370a3709,0x370c370b,0x370e370d,0x3710370f,0x37123711,0x37143713,0x37163715,
0x37173716,0x37193718,0x371b371a,0x371d371c,0x371f371e,0x37213720,0x37233722,0x37253724,0x37263725,0x37283727,0x372a3729,0x372c372b,0x372e372d,0x3730372f,0x37323731,0x37343733,
0x37353734,0x37373736,0x37393738,0x373b373a,0x373d373c,0x373f373e,0x37413740,0x37433742,0x37443743,0x37463745,0x37483747,0x374a3749,0x374c374b,0x374e374d,0x3750374f,0x37513751,
0x37533752,0x37553754,0x37573756,0x37593758,0x375b375a,0x375d375c,0x375f375e,0x37603760,0x37623761,0x37643763,0x37663765,0x37683767,0x376a3769,0x376c376b,0x376e376d,0x376f376f,
0x37733771,0x37773775,0x377a3778,0x377e377c,0x37813780,0x37833782,0x37853784,0x37873786,0x37883787,0x378a3789,0x378c378b,0x378e378d,0x3790378f,0x37923791,0x37943793,0x37963795,
0x37973796,0x37993798,0x379b379a,0x379d379c,0x379f379e,0x37a137a0,0x37a337a2,0x37a537a4,0x37a637a5,0x37a837a7,0x37aa37a9,0x37ac37ab,0x37ae37ad,0x37b037af,0x37b237b1,0x37b437b3,
0x37b537b4,0x37b737b6,0x37b937b8,0x37bb37ba,0x37bd37bc,0x37bf37be,0x37c137c0,0x37c337c2,0x37c437c3,0x37c637c5,0x37c837c7,0x37ca37c9,0x37cc37cb,0x37ce37cd,0x37d037cf,0x37d237d1,
0x37d337d2,0x37d537d4,0x37d737d6,0x37d937d8,0x37db37da,0x37dd37dc,0x37df37de,0x37e137e0,0x37e237e1,0x37e437e3,0x37e637e5,0x37e837e7,0x37ea37e9,0x37ec37eb,0x37ee37ed,0x37f037ef,
0x37f337f1,0x37f737f5,0x37fa37f8,0x37fe37fc,0x38013800,0x38033802,0x38053804,0x38073806,0x38083807,0x380a3809,0x380c380b,0x380e380d,0x3810380f,0x38123811,0x38143813,0x38163815,
0x38173816,0x38193818,0x381b381a,0x381d381c,0x381f381e,0x38213820,0x38233822,0x38253824,0x38263825,0x38283827,0x382a3829,0x382c382b,0x382e382d,0x3830382f,0x38323831,0x38343833,
0x38353834,0x38373836,0x38393838,0x383b383a,0x383d383c,0x383f383e,0x38413840,0x38433842,0x38443843,0x38463845,0x38483847,0x384a3849,0x384c384b,0x384e384d,0x3850384f,0x38523851,
0x38533852,0x38553854,0x38573856,0x38593858,0x385b385a,0x385d385c,0x385f385e,0x38613860,0x38623861,0x38643863,0x38663865,0x38683867,0x386a3869,0x386c386b,0x386e386d,0x3870386f,
0x38733871,0x38773875,0x387a3878,0x387e387c,0x38813880,0x38833882,0x38853884,0x38873886,0x38883887,0x388a3889,0x388c388b,0x388e388d,0x3890388f,0x38923891,0x38943893,0x38963895,
0x38973896,0x38993898,0x389b389a,0x389d389c,0x389f389e,0x38a138a0,0x38a338a2,0x38a538a4,0x38a638a5,0x38a838a7,0x38aa38a9,0x38ac38ab,0x38ae38ad,0x38b038af,0x38b238b1,0x38b438b3,
0x38b538b4,0x38b738b6,0x38b938b8,0x38bb38ba,0x38bd38bc,0x38bf38be,0x38c138c0,0x38c338c2,0x38c438c3,0x38c638c5,0x38c838c7,0x38ca38c9,0x38cc38cb,0x38ce38cd,0x38d038cf,0x38d238d1,
0x38d338d2,0x38d538d4,0x38d738d6,0x38d938d8,0x38db38da,0x38dd38dc,0x38df38de,0x38e138e0,0x38e238e1,0x38e438e3,0x38e638e5,0x38e838e7,0x38ea38e9,0x38ec38eb,0x38ee38ed,0x38f038ef,
0x38f338f1,0x38f738f5,0x38fa38f8,0x38fe38fc,0x39013900,0x39033902,0x39053904,0x39073906,0x39083907,0x390a3909,0x390c390b,0x390e390d,0x3910390f,0x39123911,0x39143913,0x39163915,
0x39173916,0x39193918,0x391b391a,0x391d391c,0x391f391e,0x39213920,0x39233922,0x39253924,0x39263925,0x39283927,0x392a3929,0x392c392b,0x392e392d,0x3930392f,0x39323931,0x39343933,
0x39353934,0x39373936,0x39393938,0x393b393a,0x393d393c,0x393f393e,0x39413940,0x39433942,0x39443943,0x39463945,0x39483947,0x394a3949,0x394c394b,0x394e394d,0x3950394f,0x39523951,
0x39533953,0x39553954,0x39573956,0x39593958,0x395b395a,0x395d395c,0x395f395e,0x39613960,0x39623962,0x39643963,0x39663965,0x39683967,0x396a3969,0x396c396b,0x396e396d,0x3970396f,
0x39733971,0x39773975,0x397a3978,0x397e397c,0x39813980,0x39833982,0x39853984,0x39873986,0x39883988,0x398a3989,0x398c398b,0x398e398d,0x3990398f,0x39923991,0x39943993,0x39963995,
0x39973997,0x39993998,0x399b399a,0x399d399c,0x399f399e,0x39a139a0,0x39a339a2,0x39a539a4,0x39a639a6,0x39a839a7,0x39aa39a9,0x39ac39ab,0x39ae39ad,0x39b039af,0x39b239b1,0x39b439b3,
0x39b539b5,0x39b739b6,0x39b939b8,0x39bb39ba,0x39bd39bc,0x39bf39be,0x39c139c0,0x39c339c2,0x39c439c4,0x39c639c5,0x39c839c7,0x39ca39c9,0x39cc39cb,0x39ce39cd,0x39d039cf,0x39d239d1,
0x39d439d3,0x39d539d4,0x39d739d6,0x39d939d8,0x39db39da,0x39dd39dc,0x39df39de,0x39e139e0,0x39e339e2,0x39e439e3,0x39e639e5,0x39e839e7,0x39ea39e9,0x39ec39eb,0x39ee39ed,0x39f039ef,
0x39f339f1,0x39f739f5,0x39fa39f9,0x39fe39fc,0x3a013a00,0x3a033a02,0x3a053a04,0x3a073a06,0x3a093a08,0x3a0a3a09,0x3a0c3a0b,0x3a0e3a0d,0x3a103a0f,0x3a123a11,0x3a143a13,0x3a163a15,
0x3a183a17,0x3a193a18,0x3a1b3a1a,0x3a1d3a1c,0x3a1f3a1e,0x3a213a20,0x3a233a22,0x3a253a24,0x3a273a26,0x3a283a27,0x3a2a3a29,0x3a2c3a2b,0x3a2e3a2d,0x3a303a2f,0x3a323a31,0x3a343a33,
0x3a363a35,0x3a373a37,0x3a393a38,0x3a3b3a3a,0x3a3d3a3c,0x3a3f3a3e,0x3a413a40,0x3a433a42,0x3a453a44,0x3a473a46,0x3a483a47,0x3a4a3a49,0x3a4c3a4b,0x3a4e3a4d,0x3a503a4f,0x3a523a51,
0x3a543a53,0x3a563a55,0x3a573a56,0x3a593a58,0x3a5b3a5a,0x3a5d3a5c,0x3a5f3a5e,0x3a613a60,0x3a633a62,0x3a653a64,0x3a663a66,0x3a683a67,0x3a6a3a69,0x3a6c3a6b,0x3a6e3a6d,0x3a703a6f,
0x3a733a71,0x3a773a75,0x3a7b3a79,0x3a7e3a7d,0x3a813a80,0x3a833a82,0x3a853a84,0x3a873a86,0x3a893a88,0x3a8b3a8a,0x3a8c3a8b,0x3a8e3a8d,0x3a903a8f,0x3a923a91,0x3a943a93,0x3a963a95,
0x3a983a97,0x3a9a3a99,0x3a9b3a9b,0x3a9d3a9c,0x3a9f3a9e,0x3aa13aa0,0x3aa33aa2,0x3aa53aa4,0x3aa73aa6,0x3aa93aa8,0x3aab3aaa,0x3aac3aab,0x3aae3aad,0x3ab03aaf,0x3ab23ab1,0x3ab43ab3,
0x3ab63ab5,0x3ab83ab7,0x3aba3ab9,0x3abb3abb,0x3abd3abc,0x3abf3abe,0x3ac13ac0,0x3ac33ac2,0x3ac53ac4,0x3ac73ac6,0x3ac93ac8,0x3acb3aca,0x3acc3acb,0x3ace3acd,0x3ad03acf,0x3ad23ad1,
0x3ad43ad3,0x3ad63ad5,0x3ad83ad7,0x3ada3ad9,0x3adb3adb,0x3add3adc,0x3adf3ade,0x3ae13ae0,0x3ae33ae2,0x3ae53ae4,0x3ae73ae6,0x3ae93ae8,0x3aeb3aea,0x3aec3aec,0x3aee3aed,0x3af03aef,
0x3af43af2,0x3af73af5,0x3afb3af9,0x3aff3afd,0x3b013b00,0x3b033b02,0x3b053b04,0x3b073b06,0x3b093b08,0x3b0b3b0a,0x3b0d3b0c,0x3b0f3b0e,0x3b103b0f,0x3b123b11,0x3b143b13,0x3b163b15,
0x3b183b17,0x3b1a3b19,0x3b1c3b1b,0x3b1e3b1d,0x3b203b1f,0x3b213b20,0x3b233b22,0x3b253b24,0x3b273b26,0x3b293b28,0x3b2b3b2a,0x3b2d3b2c,0x3b2f3b2e,0x3b313b30,0x3b323b31,0x3b343b33,
0x3b363b35,0x3b383b37,0x3b3a3b39,0x3b3c3b3b,0x3b3e3b3d,0x3b403b3f,0x3b423b41,0x3b433b43,0x3b453b44,0x3b473b46,0x3b493b48,0x3b4b3b4a,0x3b4d3b4c,0x3b4f3b4e,0x3b513b50,0x3b533b52,
0x3b553b54,0x3b563b55,0x3b583b57,0x3b5a3b59,0x3b5c3b5b,0x3b5e3b5d,0x3b603b5f,0x3b623b61,0x3b643b63,0x3b663b65,0x3b673b67,0x3b693b68,0x3b6b3b6a,0x3b6d3b6c,0x3b6f3b6e,0x3b713b70,
0x3b743b72,0x3b783b76,0x3b7c3b7a,0x3b803b7e,0x3b823b81,0x3b843b83,0x3b863b85,0x3b873b86,0x3b893b88,0x3b8b3b8a,0x3b8d3b8c,0x3b8f3b8e,0x3b913b90,0x3b933b92,0x3b953b94,0x3b973b96,
0x3b993b98,0x3b9a3b9a,0x3b9c3b9b,0x3b9e3b9d,0x3ba03b9f,0x3ba23ba1,0x3ba43ba3,0x3ba63ba5,0x3ba83ba7,0x3baa3ba9,0x3bac3bab,0x3bae3bad,0x3baf3bae,0x3bb13bb0,0x3bb33bb2,0x3bb53bb4,
0x3bb73bb6,0x3bb93bb8,0x3bbb3bba,0x3bbd3bbc,0x3bbf3bbe,0x3bc13bc0,0x3bc33bc2,0x3bc43bc4,0x3bc63bc5,0x3bc83bc7,0x3bca3bc9,0x3bcc3bcb,0x3bce3bcd,0x3bd03bcf,0x3bd23bd1,0x3bd43bd3,
0x3bd63bd5,0x3bd83bd7,0x3bda3bd9,0x3bdb3bda,0x3bdd3bdc,0x3bdf3bde,0x3be13be0,0x3be33be2,0x3be53be4,0x3be73be6,0x3be93be8,0x3beb3bea,0x3bed3bec,0x3bef3bee,0x3bf13bf0,0x3bf23bf2,
0x3bf63bf4,0x3bfa3bf8,0x3bfe3bfc,0x3c013bff,0x3c033c02,0x3c053c04,0x3c063c05,0x3c083c07,0x3c0a3c09,0x3c0c3c0b,0x3c0e3c0d,0x3c103c0f,0x3c123c11,0x3c143c13,0x3c163c15,0x3c183c17,
0x3c1a3c19,0x3c1c3c1b,0x3c1e3c1d,0x3c203c1f,0x3c223c21,0x3c233c22,0x3c253c24,0x3c273c26,0x3c293c28,0x3c2b3c2a,0x3c2d3c2c,0x3c2f3c2e,0x3c313c30,0x3c333c32,0x3c353c34,0x3c373c36,
0x3c393c38,0x3c3b3c3a,0x3c3d3c3c,0x3c3f3c3e,0x3c413c40,0x3c433c42,0x3c443c44,0x3c463c45,0x3c483c47,0x3c4a3c49,0x3c4c3c4b,0x3c4e3c4d,0x3c503c4f,0x3c523c51,0x3c543c53,0x3c563c55,
0x3c583c57,0x3c5a3c59,0x3c5c3c5b,0x3c5e3c5d,0x3c603c5f,0x3c623c61,0x3c643c63,0x3c663c65,0x3c683c67,0x3c6a3c69,0x3c6c3c6b,0x3c6e3c6d,0x3c703c6f,0x3c723c71,0x3c733c72,0x3c753c74,
0x3c793c77,0x3c7d3c7b,0x3c803c7f,0x3c823c81,0x3c843c83,0x3c863c85,0x3c883c87,0x3c8a3c89,0x3c8c3c8b,0x3c8e3c8d,0x3c903c8f,0x3c923c91,0x3c943c93,0x3c963c95,0x3c983c97,0x3c9a3c99,
0x3c9c3c9b,0x3c9e3c9d,0x3ca03c9f,0x3ca23ca1,0x3ca43ca3,0x3ca63ca5,0x3ca83ca7,0x3caa3ca9,0x3cac3cab,0x3cae3cad,0x3cb03caf,0x3cb23cb1,0x3cb43cb3,0x3cb63cb5,0x3cb83cb7,0x3cba3cb9,
0x3cbc3cbb,0x3cbe3cbd,0x3cc03cbf,0x3cc23cc1,0x3cc43cc3,0x3cc63cc5,0x3cc83cc7,0x3cca3cc9,0x3ccc3ccb,0x3cce3ccd,0x3cd03ccf,0x3cd23cd1,0x3cd53cd3,0x3cd73cd6,0x3cd93cd8,0x3cdb3cda,
0x3cdd3cdc,0x3cdf3cde,0x3ce13ce0,0x3ce33ce2,0x3ce53ce4,0x3ce73ce6,0x3ce93ce8,0x3ceb3cea,0x3ced3cec,0x3cef3cee,0x3cf13cf0,0x3cf33cf2,0x3cf53cf4,0x3cf73cf6,0x3cf93cf8,0x3cfb3cfa,
0x3cff3cfd,0x3d023d01,0x3d043d03,0x3d063d05,0x3d083d07,0x3d0a3d09,0x3d0c3d0b,0x3d0e3d0d,0x3d103d0f,0x3d123d11,0x3d143d13,0x3d163d15,0x3d183d17,0x3d1b3d1a,0x3d1d3d1c,0x3d1f3d1e,
0x3d213d20,0x3d233d22,0x3d253d24,0x3d273d26,0x3d293d28,0x3d2b3d2a,0x3d2e3d2d,0x3d303d2f,0x3d323d31,0x3d343d33,0x3d363d35,0x3d383d37,0x3d3a3d39,0x3d3d3d3b,0x3d3f3d3e,0x3d413d40,
0x3d433d42,0x3d453d44,0x3d473d46,0x3d4a3d48,0x3d4c3d4b,0x3d4e3d4d,0x3d503d4f,0x3d523d51,0x3d543d53,0x3d573d55,0x3d593d58,0x3d5b3d5a,0x3d5d3d5c,0x3d5f3d5e,0x3d623d60,0x3d643d63,
0x3d663d65,0x3d683d67,0x3d6a3d69,0x3d6d3d6b,0x3d6f3d6e,0x3d713d70,0x3d733d72,0x3d753d74,0x3d783d77,0x3d7a3d79,0x3d7c3d7b,0x3d7e3d7d,0x3d803d7f,0x3d813d81,0x3d833d82,0x3d843d83,
0x3d863d84,0x3d883d87,0x3d8a3d89,0x3d8c3d8b,0x3d8f3d8e,0x3d913d90,0x3d933d92,0x3d953d94,0x3d983d97,0x3d9a3d99,0x3d9c3d9b,0x3d9f3d9e,0x3da13da0,0x3da33da2,0x3da63da5,0x3da83da7,
0x3daa3da9,0x3dad3dac,0x3daf3dae,0x3db13db0,0x3db43db3,0x3db63db5,0x3db93db7,0x3dbb3dba,0x3dbd3dbc,0x3dc03dbe,0x3dc23dc1,0x3dc43dc3,0x3dc73dc6,0x3dc93dc8,0x3dcc3dca,0x3dce3dcd,
0x3dd13dcf,0x3dd33dd2,0x3dd53dd4,0x3dd83dd7,0x3dda3dd9,0x3ddd3ddc,0x3ddf3dde,0x3de23de0,0x3de43de3,0x3de73de5,0x3de93de8,0x3dec3dea,0x3dee3ded,0x3df13def,0x3df33df2,0x3df63df4,
0x3df83df7,0x3dfb3df9,0x3dfd3dfc,0x3e003dff,0x3e013e01,0x3e023e02,0x3e043e03,0x3e053e04,0x3e063e06,0x3e083e07,0x3e093e08,0x3e0a3e0a,0x3e0b3e0b,0x3e0d3e0c,0x3e0e3e0d,0x3e0f3e0f,
0x3e123e10,0x3e143e13,0x3e173e16,0x3e1a3e18,0x3e1c3e1b,0x3e1f3e1e,0x3e223e20,0x3e243e23,0x3e273e26,0x3e2a3e28,0x3e2c3e2b,0x3e2f3e2e,0x3e323e30,0x3e353e33,0x3e373e36,0x3e3a3e39,
0x3e3d3e3b,0x3e403e3e,0x3e423e41,0x3e453e44,0x3e483e47,0x3e4b3e4a,0x3e4e3e4c,0x3e513e4f,0x3e533e52,0x3e563e55,0x3e593e58,0x3e5c3e5b,0x3e5f3e5e,0x3e623e61,0x3e653e63,0x3e683e66,
0x3e6b3e69,0x3e6e3e6c,0x3e713e6f,0x3e743e72,0x3e773e75,0x3e7a3e78,0x3e7d3e7b,0x3e803e7e,0x3e813e81,0x3e833e82,0x3e843e84,0x3e863e85,0x3e873e87,0x3e893e88,0x3e8b3e8a,0x3e8c3e8b,
0x3e8e3e8d,0x3e8f3e8e,0x3e913e90,0x3e923e92,0x3e943e93,0x3e953e95,0x3e973e96,0x3e993e98,0x3e9a3e99,0x3e9c3e9b,0x3e9d3e9d,0x3e9f3e9e,0x3ea13ea0,0x3ea23ea1,0x3ea43ea3,0x3ea63ea5,
0x3ea83ea7,0x3eac3eaa,0x3eaf3ead,0x3eb23eb1,0x3eb63eb4,0x3eb93eb7,0x3ebc3ebb,0x3ec03ebe,0x3ec33ec1,0x3ec63ec5,0x3eca3ec8,0x3ecd3ecc,0x3ed13ecf,0x3ed43ed3,0x3ed83ed6,0x3edb3eda,
0x3edf3edd,0x3ee23ee1,0x3ee63ee4,0x3ee93ee8,0x3eed3eeb,0x3ef13eef,0x3ef43ef2,0x3ef83ef6,0x3efc3efa,0x3eff3efd,0x3f013f01,0x3f033f02,0x3f053f04,0x3f073f06,0x3f093f08,0x3f0b3f0a,
0x3f0d3f0c,0x3f0e3f0e,0x3f103f0f,0x3f123f11,0x3f143f13,0x3f163f15,0x3f183f17,0x3f1a3f19,0x3f1c3f1b,0x3f1e3f1d,0x3f203f1f,0x3f223f21,0x3f243f23,0x3f253f25,0x3f273f26,0x3f293f28,
0x3f2b3f2a,0x3f2d3f2c,0x3f2f3f2e,0x3f313f30,0x3f333f32,0x3f353f34,0x3f373f36,0x3f393f38,0x3f3b3f3a,0x3f3d3f3c,0x3f3f3f3e,0x3f413f40,0x3f433f42,0x3f453f44,0x3f473f46,0x3f493f48,
0x3f4d3f4b,0x3f513f4f,0x3f553f53,0x3f593f57,0x3f5d3f5b,0x3f613f5f,0x3f663f63,0x3f6a3f68,0x3f6e3f6c,0x3f723f70,0x3f763f74,0x3f7a3f78,0x3f7f3f7c,0x3f813f80,0x3f833f82,0x3f863f85,
0x3f883f87,0x3f8a3f89,0x3f8c3f8b,0x3f8e3f8d,0x3f903f8f,0x3f923f91,0x3f943f93,0x3f963f95,0x3f993f98,0x3f9b3f9a,0x3f9d3f9c,0x3f9f3f9e,0x3fa13fa0,0x3fa33fa2,0x3fa53fa4,0x3fa73fa6,
0x3faa3fa8,0x3fac3fab,0x3fae3fad,0x3fb03faf,0x3fb23fb1,0x3fb43fb3,0x3fb63fb5,0x3fb83fb7,0x3fba3fb9,0x3fbc3fbb,0x3fbf3fbe,0x3fc13fc0,0x3fc33fc2,0x3fc53fc4,0x3fc73fc6,0x3fc93fc8,
0x3fcb3fca,0x3fcd3fcc,0x3fcf3fce,0x3fd13fd0,0x3fd33fd2,0x3fd53fd4,0x3fd83fd7,0x3fda3fd9,0x3fdc3fdb,0x3fde3fdd,0x3fe03fdf,0x3fe23fe1,0x3fe43fe3,0x3fe63fe5,0x3fe83fe7,0x3fea3fe9,
0x3fee3fec,0x3ff23ff0,0x3ff63ff4,0x3ffa3ff8,0x3ffe3ffc,0x40014000,0x40034002,0x40054004,0x40074006,0x40094008,0x400b400a,0x400d400c,0x400f400e,0x40114010,0x40134012,0x40154014,
0x40174016,0x40184017,0x401a4019,0x401c401b,0x401e401d,0x4020401f,0x40224021,0x40244023,0x40264025,0x40284027,0x402a4029,0x402c402b,0x402e402d,0x402f402f,0x40314030,0x40334032,
0x40354034,0x40374036,0x40394038,0x403b403a,0x403d403c,0x403f403e,0x40414040,0x40424041,0x40444043,0x40464045,0x40484047,0x404a4049,0x404c404b,0x404e404d,0x4050404f,0x40514051,
0x40534052,0x40554054,0x40574056,0x40594058,0x405b405a,0x405d405c,0x405f405e,0x40604060,0x40624061,0x40644063,0x40664065,0x40684067,0x406a4069,0x406c406b,0x406e406d,0x406f406f,
0x40734071,0x40774075,0x407a4078,0x407e407c,0x40814080,0x40834082,0x40854084,0x40874086,0x40884087,0x408a4089,0x408c408b,0x408e408d,0x4090408f,0x40924091,0x40944093,0x40964095,
0x40974096,0x40994098,0x409b409a,0x409d409c,0x409f409e,0x40a140a0,0x40a340a2,0x40a540a4,0x40a640a5,0x40a840a7,0x40aa40a9,0x40ac40ab,0x40ae40ad,0x40b040af,0x40b240b1,0x40b440b3,
0x40b540b4,0x40b740b6,0x40b940b8,0x40bb40ba,0x40bd40bc,0x40bf40be,0x40c140c0,0x40c240c2,0x40c440c3,0x40c640c5,0x40c840c7,0x40ca40c9,0x40cc40cb,0x40ce40cd,0x40d040cf,0x40d140d1,
0x40d340d2,0x40d540d4,0x40d740d6,0x40d940d8,0x40db40da,0x40dd40dc,0x40df40de,0x40e040e0,0x40e240e1,0x40e440e3,0x40e640e5,0x40e840e7,0x40ea40e9,0x40ec40eb,0x40ee40ed,0x40ef40ef,
0x40f340f1,0x40f740f5,0x40fa40f8,0x40fe40fc,0x41014100,0x41034102,0x41054104,0x41074106,0x41084107,0x410a4109,0x410c410b,0x410e410d,0x4110410f,0x41124111,0x41144113,0x41164115,
0x41174116,0x41194118,0x411b411a,0x411d411c,0x411f411e,0x41214120,0x41234122,0x41254124,0x41264125,0x41284127,0x412a4129,0x412c412b,0x412e412d,0x4130412f,0x41324131,0x41344133,
0x41354134,0x41374136,0x41394138,0x413b413a,0x413d413c,0x413f413e,0x41414140,0x41424142,0x41444143,0x41464145,0x41484147,0x414a4149,0x414c414b,0x414e414d,0x4150414f,0x41514151,
0x41534152,0x41554154,0x41574156,0x41594158,0x415b415a,0x415d415c,0x415f415e,0x41604160,0x41624161,0x41644163,0x41664165,0x41684167,0x416a4169,0x416c416b,0x416e416d,0x416f416f,
0x41734171,0x41774175,0x417a4178,0x417e417c,0x41814180,0x41834182,0x41854184,0x41874186,0x41884187,0x418a4189,0x418c418b,0x418e418d,0x4190418f,0x41924191,0x41944193,0x41964195,
0x41974196,0x41994198,0x419b419a,0x419d419c,0x419f419e,0x41a141a0,0x41a341a2,0x41a541a4,0x41a641a5,0x41a841a7,0x41aa41a9,0x41ac41ab,0x41ae41ad,0x41b041af,0x41b241b1,0x41b441b3,
0x41b541b4,0x41b741b6,0x41b941b8,0x41bb41ba,0x41bd41bc,0x41bf41be,0x41c141c0,0x41c241c2,0x41c441c3,0x41c641c5,0x41c841c7,0x41ca41c9,0x41cc41cb,0x41ce41cd,0x41d041cf,0x41d141d1,
0x41d341d2,0x41d541d4,0x41d741d6,0x41d941d8,0x41db41da,0x41dd41dc,0x41df41de,0x41e041e0,0x41e241e1,0x41e441e3,0x41e641e5,0x41e841e7,0x41ea41e9,0x41ec41eb,0x41ee41ed,0x41ef41ef,
0x41f341f1,0x41f741f5,0x41fa41f8,0x41fe41fc,0x42014200,0x42034202,0x42054204,0x42074206,0x42084207,0x420a4209,0x420c420b,0x420e420d,0x4210420f,0x42124211,0x42144213,0x42164215,
0x42174216,0x42194218,0x421b421a,0x421d421c,0x421f421e,0x42214220,0x42234222,0x42254224,0x42264225,0x42284227,0x422a4229,0x422c422b,0x422e422d,0x4230422f,0x42324231,0x42344233,
0x42354234,0x42374236,0x42394238,0x423b423a,0x423d423c,0x423f423e,0x42414240,0x42424242,0x42444243,0x42464245,0x42484247,0x424a4249,0x424c424b,0x424e424d,0x4250424f,0x42514251,
0x42534252,0x42554254,0x42574256,0x42594258,0x425b425a,0x425d425c,0x425f425e,0x42604260,0x42624261,0x42644263,0x42664265,0x42684267,0x426a4269,0x426c426b,0x426e426d,0x426f426f,
0x42734271,0x42774275,0x427a4278,0x427e427c,0x42814280,0x42834282,0x42854284,0x42874286,0x42884287,0x428a4289,0x428c428b,0x428e428d,0x4290428f,0x42924291,0x42944293,0x42964295,
0x42974296,0x42994298,0x429b429a,0x429d429c,0x429f429e,0x42a142a0,0x42a342a2,0x42a542a4,0x42a642a5,0x42a842a7,0x42aa42a9,0x42ac42ab,0x42ae42ad,0x42b042af,0x42b242b1,0x42b442b3,
0x42b542b4,0x42b742b6,0x42b942b8,0x42bb42ba,0x42bd42bc,0x42bf42be,0x42c142c0,0x42c242c2,0x42c442c3,0x42c642c5,0x42c842c7,0x42ca42c9,0x42cc42cb,0x42ce42cd,0x42d042cf,0x42d142d1,
0x42d342d2,0x42d542d4,0x42d742d6,0x42d942d8,0x42db42da,0x42dd42dc,0x42df42de,0x42e042e0,0x42e242e1,0x42e442e3,0x42e642e5,0x42e842e7,0x42ea42e9,0x42ec42eb,0x42ee42ed,0x42ef42ef,
0x42f342f1,0x42f742f5,0x42fa42f8,0x42fe42fc,0x43014300,0x43034302,0x43054304,0x43074306,0x43084307,0x430a4309,0x430c430b,0x430e430d,0x4310430f,0x43124311,0x43144313,0x43164315,
0x43174316,0x43194318,0x431b431a,0x431d431c,0x431f431e,0x43214320,0x43234322,0x43254324,0x43264325,0x43284327,0x432a4329,0x432c432b,0x432e432d,0x4330432f,0x43324331,0x43344333,
0x43354334,0x43374336,0x43394338,0x433b433a,0x433d433c,0x433f433e,0x43414340,0x43424342,0x43444343,0x43464345,0x43484347,0x434a4349,0x434c434b,0x434e434d,0x4350434f,0x43514351,
0x43534352,0x43554354,0x43574356,0x43594358,0x435b435a,0x435d435c,0x435f435e,0x43604360,0x43624361,0x43644363,0x43664365,0x43684367,0x436a4369,0x436c436b,0x436e436d,0x436f436f,
0x43734371,0x43774375,0x437a4378,0x437e437c,0x43814380,0x43834382,0x43854384,0x43874386,0x43884387,0x438a4389,0x438c438b,0x438e438d,0x4390438f,0x43924391,0x43944393,0x43964395,
0x43974396,0x43994398,0x439b439a,0x439d439c,0x439f439e,0x43a143a0,0x43a343a2,0x43a543a4,0x43a643a5,0x43a843a7,0x43aa43a9,0x43ac43ab,0x43ae43ad,0x43b043af,0x43b243b1,0x43b443b3,
0x43b543b4,0x43b743b6,0x43b943b8,0x43bb43ba,0x43bd43bc,0x43bf43be,0x43c143c0,0x43c243c2,0x43c443c3,0x43c643c5,0x43c843c7,0x43ca43c9,0x43cc43cb,0x43ce43cd,0x43d043cf,0x43d143d1,
0x43d343d2,0x43d543d4,0x43d743d6,0x43d943d8,0x43db43da,0x43dd43dc,0x43df43de,0x43e043e0,0x43e243e1,0x43e443e3,0x43e643e5,0x43e843e7,0x43ea43e9,0x43ec43eb,0x43ee43ed,0x43ef43ef,
0xb373b371,0xb377b375,0xb37ab378,0xb37eb37c,0xb381b380,0xb383b382,0xb385b384,0xb387b386,0xb388b387,0xb38ab389,0xb38cb38b,0xb38eb38d,0xb390b38f,0xb392b391,0xb394b393,0xb396b395,
0xb397b396,0xb399b398,0xb39bb39a,0xb39db39c,0xb39fb39e,0xb3a1b3a0,0xb3a3b3a2,0xb3a5b3a4,0xb3a6b3a5,0xb3a8b3a7,0xb3aab3a9,0xb3acb3ab,0xb3aeb3ad,0xb3b0b3af,0xb3b2b3b1,0xb3b4b3b3,
0xb3b5b3b4,0xb3b7b3b6,0xb3b9b3b8,0xb3bbb3ba,0xb3bdb3bc,0xb3bfb3be,0xb3c1b3c0,0xb3c2b3c2,0xb3c4b3c3,0xb3c6b3c5,0xb3c8b3c7,0xb3cab3c9,0xb3ccb3cb,0xb3ceb3cd,0xb3d0b3cf,0xb3d1b3d1,
0xb3d3b3d2,0xb3d5b3d4,0xb3d7b3d6,0xb3d9b3d8,0xb3dbb3da,0xb3ddb3dc,0xb3dfb3de,0xb3e0b3e0,0xb3e2b3e1,0xb3e4b3e3,0xb3e6b3e5,0xb3e8b3e7,0xb3eab3e9,0xb3ecb3eb,0xb3eeb3ed,0xb3efb3ef,
0xb3f3b3f1,0xb3f7b3f5,0xb3fab3f8,0xb3feb3fc,0xb401b400,0xb403b402,0xb405b404,0xb407b406,0xb408b407,0xb40ab409,0xb40cb40b,0xb40eb40d,0xb410b40f,0xb412b411,0xb414b413,0xb416b415,
0xb417b416,0xb419b418,0xb41bb41a,0xb41db41c,0xb41fb41e,0xb421b420,0xb423b422,0xb425b424,0xb426b425,0xb428b427,0xb42ab429,0xb42cb42b,0xb42eb42d,0xb430b42f,0xb432b431,0xb434b433,
0xb435b434,0xb437b436,0xb439b438,0xb43bb43a,0xb43db43c,0xb43fb43e,0xb441b440,0xb442b442,0xb444b443,0xb446b445,0xb448b447,0xb44ab449,0xb44cb44b,0xb44eb44d,0xb450b44f,0xb451b451,
0xb453b452,0xb455b454,0xb457b456,0xb459b458,0xb45bb45a,0xb45db45c,0xb45fb45e,0xb460b460,0xb462b461,0xb464b463,0xb466b465,0xb468b467,0xb46ab469,0xb46cb46b,0xb46eb46d,0xb46fb46f,
0xb473b471,0xb477b475,0xb47ab478,0xb47eb47c,0xb481b480,0xb483b482,0xb485b484,0xb487b486,0xb488b487,0xb48ab489,0xb48cb48b,0xb48eb48d,0xb490b48f,0xb492b491,0xb494b493,0xb496b495,
0xb497b496,0xb499b498,0xb49bb49a,0xb49db49c,0xb49fb49e,0xb4a1b4a0,0xb4a3b4a2,0xb4a5b4a4,0xb4a6b4a5,0xb4a8b4a7,0xb4aab4a9,0xb4acb4ab,0xb4aeb4ad,0xb4b0b4af,0xb4b2b4b1,0xb4b4b4b3,
0xb4b5b4b4,0xb4b7b4b6,0xb4b9b4b8,0xb4bbb4ba,0xb4bdb4bc,0xb4bfb4be,0xb4c1b4c0,0xb4c2b4c2,0xb4c4b4c3,0xb4c6b4c5,0xb4c8b4c7,0xb4cab4c9,0xb4ccb4cb,0xb4ceb4cd,0xb4d0b4cf,0xb4d1b4d1,
0xb4d3b4d2,0xb4d5b4d4,0xb4d7b4d6,0xb4d9b4d8,0xb4dbb4da,0xb4ddb4dc,0xb4dfb4de,0xb4e0b4e0,0xb4e2b4e1,0xb4e4b4e3,0xb4e6b4e5,0xb4e8b4e7,0xb4eab4e9,0xb4ecb4eb,0xb4eeb4ed,0xb4efb4ef,
0xb4f3b4f1,0xb4f7b4f5,0xb4fab4f8,0xb4feb4fc,0xb501b500,0xb503b502,0xb505b504,0xb507b506,0xb508b507,0xb50ab509,0xb50cb50b,0xb50eb50d,0xb510b50f,0xb512b511,0xb514b513,0xb516b515,
0xb517b516,0xb519b518,0xb51bb51a,0xb51db51c,0xb51fb51e,0xb521b520,0xb523b522,0xb525b524,0xb526b525,0xb528b527,0xb52ab529,0xb52cb52b,0xb52eb52d,0xb530b52f,0xb532b531,0xb533b533,
0xb535b534,0xb537b536,0xb539b538,0xb53bb53a,0xb53db53c,0xb53fb53e,0xb541b540,0xb542b542,0xb544b543,0xb546b545,0xb548b547,0xb54ab549,0xb54cb54b,0xb54eb54d,0xb550b54f,0xb551b551,
0xb553b552,0xb555b554,0xb557b556,0xb559b558,0xb55bb55a,0xb55db55c,0xb55fb55e,0xb560b560,0xb562b561,0xb564b563,0xb566b565,0xb568b567,0xb56ab569,0xb56cb56b,0xb56eb56d,0xb56fb56f,
0xb573b571,0xb577b575,0xb57ab578,0xb57eb57c,0xb581b580,0xb583b582,0xb585b584,0xb587b586,0xb588b587,0xb58ab589,0xb58cb58b,0xb58eb58d,0xb590b58f,0xb592b591,0xb594b593,0xb596b595,
0xb597b596,0xb599b598,0xb59bb59a,0xb59db59c,0xb59fb59e,0xb5a1b5a0,0xb5a3b5a2,0xb5a5b5a4,0xb5a6b5a5,0xb5a8b5a7,0xb5aab5a9,0xb5acb5ab,0xb5aeb5ad,0xb5b0b5af,0xb5b2b5b1,0xb5b3b5b3,
0xb5b5b5b4,0xb5b7b5b6,0xb5b9b5b8,0xb5bbb5ba,0xb5bdb5bc,0xb5bfb5be,0xb5c1b5c0,0xb5c2b5c2,0xb5c4b5c3,0xb5c6b5c5,0xb5c8b5c7,0xb5cab5c9,0xb5ccb5cb,0xb5ceb5cd,0xb5d0b5cf,0xb5d1b5d1,
0xb5d3b5d2,0xb5d5b5d4,0xb5d7b5d6,0xb5d9b5d8,0xb5dbb5da,0xb5ddb5dc,0xb5dfb5de,0xb5e0b5e0,0xb5e2b5e1,0xb5e4b5e3,0xb5e6b5e5,0xb5e8b5e7,0xb5eab5e9,0xb5ecb5eb,0xb5eeb5ed,0xb5efb5ef,
0xb5f3b5f1,0xb5f7b5f5,0xb5fab5f8,0xb5feb5fc,0xb601b600,0xb603b602,0xb605b604,0xb607b606,0xb608b607,0xb60ab609,0xb60cb60b,0xb60eb60d,0xb610b60f,0xb612b611,0xb614b613,0xb616b615,
0xb617b616,0xb619b618,0xb61bb61a,0xb61db61c,0xb61fb61e,0xb621b620,0xb623b622,0xb625b624,0xb626b625,0xb628b627,0xb62ab629,0xb62cb62b,0xb62eb62d,0xb630b62f,0xb632b631,0xb633b633,
0xb635b634,0xb637b636,0xb639b638,0xb63bb63a,0xb63db63c,0xb63fb63e,0xb641b640,0xb642b642,0xb644b643,0xb646b645,0xb648b647,0xb64ab649,0xb64cb64b,0xb64eb64d,0xb650b64f,0xb651b651,
0xb653b652,0xb655b654,0xb657b656,0xb659b658,0xb65bb65a,0xb65db65c,0xb65fb65e,0xb660b660,0xb662b661,0xb664b663,0xb666b665,0xb668b667,0xb66ab669,0xb66cb66b,0xb66eb66d,0xb66fb66f,
0xb673b671,0xb677b675,0xb67ab678,0xb67eb67c,0xb681b680,0xb683b682,0xb685b684,0xb687b686,0xb688b687,0xb68ab689,0xb68cb68b,0xb68eb68d,0xb690b68f,0xb692b691,0xb694b693,0xb696b695,
0xb697b696,0xb699b698,0xb69bb69a,0xb69db69c,0xb69fb69e,0xb6a1b6a0,0xb6a3b6a2,0xb6a5b6a4,0xb6a6b6a5,0xb6a8b6a7,0xb6aab6a9,0xb6acb6ab,0xb6aeb6ad,0xb6b0b6af,0xb6b2b6b1,0xb6b3b6b3,
0xb6b5b6b4,0xb6b7b6b6,0xb6b9b6b8,0xb6bbb6ba,0xb6bdb6bc,0xb6bfb6be,0xb6c1b6c0,0xb6c2b6c2,0xb6c4b6c3,0xb6c6b6c5,0xb6c8b6c7,0xb6cab6c9,0xb6ccb6cb,0xb6ceb6cd,0xb6d0b6cf,0xb6d1b6d1,
0xb6d3b6d2,0xb6d5b6d4,0xb6d7b6d6,0xb6d9b6d8,0xb6dbb6da,0xb6ddb6dc,0xb6dfb6de,0xb6e0b6e0,0xb6e2b6e1,0xb6e4b6e3,0xb6e6b6e5,0xb6e8b6e7,0xb6eab6e9,0xb6ecb6eb,0xb6eeb6ed,0xb6efb6ef,
0xb6f3b6f1,0xb6f7b6f5,0xb6fab6f8,0xb6feb6fc,0xb701b700,0xb703b702,0xb705b704,0xb707b706,0xb708b707,0xb70ab709,0xb70cb70b,0xb70eb70d,0xb710b70f,0xb712b711,0xb714b713,0xb716b715,
0xb717b716,0xb719b718,0xb71bb71a,0xb71db71c,0xb71fb71e,0xb721b720,0xb723b722,0xb724b724,0xb726b725,0xb728b727,0xb72ab729,0xb72cb72b,0xb72eb72d,0xb730b72f,0xb732b731,0xb733b733,
0xb735b734,0xb737b736,0xb739b738,0xb73bb73a,0xb73db73c,0xb73fb73e,0xb741b740,0xb742b742,0xb744b743,0xb746b745,0xb748b747,0xb74ab749,0xb74cb74b,0xb74eb74d,0xb750b74f,0xb751b751,
0xb753b752,0xb755b754,0xb757b756,0xb759b758,0xb75bb75a,0xb75db75c,0xb75fb75e,0xb760b760,0xb762b761,0xb764b763,0xb766b765,0xb768b767,0xb76ab769,0xb76cb76b,0xb76eb76d,0xb76fb76f,
0xb773b771,0xb777b775,0xb77ab778,0xb77eb77c,0xb781b780,0xb783b782,0xb785b784,0xb787b786,0xb788b787,0xb78ab789,0xb78cb78b,0xb78eb78d,0xb790b78f,0xb792b791,0xb794b793,0xb796b795,
0xb797b796,0xb799b798,0xb79bb79a,0xb79db79c,0xb79fb79e,0xb7a1b7a0,0xb7a3b7a2,0xb7a4b7a4,0xb7a6b7a5,0xb7a8b7a7,0xb7aab7a9,0xb7acb7ab,0xb7aeb7ad,0xb7b0b7af,0xb7b2b7b1,0xb7b3b7b3,
0xb7b5b7b4,0xb7b7b7b6,0xb7b9b7b8,0xb7bbb7ba,0xb7bdb7bc,0xb7bfb7be,0xb7c1b7c0,0xb7c2b7c2,0xb7c4b7c3,0xb7c6b7c5,0xb7c8b7c7,0xb7cab7c9,0xb7ccb7cb,0xb7ceb7cd,0xb7d0b7cf,0xb7d1b7d1,
0xb7d3b7d2,0xb7d5b7d4,0xb7d7b7d6,0xb7d9b7d8,0xb7dbb7da,0xb7ddb7dc,0xb7dfb7de,0xb7e0b7e0,0xb7e2b7e1,0xb7e4b7e3,0xb7e6b7e5,0xb7e8b7e7,0xb7eab7e9,0xb7ecb7eb,0xb7eeb7ed,0xb7efb7ef,
0xb7f3b7f1,0xb7f7b7f5,0xb7fab7f8,0xb7feb7fc,0xb801b800,0xb803b802,0xb805b804,0xb807b806,0xb808b807,0xb80ab809,0xb80cb80b,0xb80eb80d,0xb810b80f,0xb812b811,0xb814b813,0xb815b815,
0xb817b816,0xb819b818,0xb81bb81a,0xb81db81c,0xb81fb81e,0xb821b820,0xb823b822,0xb824b824,0xb826b825,0xb828b827,0xb82ab829,0xb82cb82b,0xb82eb82d,0xb830b82f,0xb832b831,0xb833b833,
0xb835b834,0xb837b836,0xb839b838,0xb83bb83a,0xb83db83c,0xb83fb83e,0xb841b840,0xb842b842,0xb844b843,0xb846b845,0xb848b847,0xb84ab849,0xb84cb84b,0xb84eb84d,0xb850b84f,0xb851b851,
0xb853b852,0xb855b854,0xb857b856,0xb859b858,0xb85bb85a,0xb85db85c,0xb85fb85e,0xb860b860,0xb862b861,0xb864b863,0xb866b865,0xb868b867,0xb86ab869,0xb86cb86b,0xb86eb86d,0xb86fb86f,
0xb873b871,0xb876b875,0xb87ab878,0xb87eb87c,0xb881b880,0xb883b882,0xb885b884,0xb886b886,0xb888b887,0xb88ab889,0xb88cb88b,0xb88eb88d,0xb890b88f,0xb892b891,0xb894b893,0xb895b895,
0xb897b896,0xb899b898,0xb89bb89a,0xb89db89c,0xb89fb89e,0xb8a1b8a0,0xb8a3b8a2,0xb8a4b8a4,0xb8a6b8a5,0xb8a8b8a7,0xb8aab8a9,0xb8acb8ab,0xb8aeb8ad,0xb8b0b8af,0xb8b2b8b1,0xb8b3b8b3,
0xb8b5b8b4,0xb8b7b8b6,0xb8b9b8b8,0xb8bbb8ba,0xb8bdb8bc,0xb8bfb8be,0xb8c1b8c0,0xb8c2b8c2,0xb8c4b8c3,0xb8c6b8c5,0xb8c8b8c7,0xb8cab8c9,0xb8ccb8cb,0xb8ceb8cd,0xb8d0b8cf,0xb8d1b8d1,
0xb8d3b8d2,0xb8d5b8d4,0xb8d7b8d6,0xb8d9b8d8,0xb8dbb8da,0xb8ddb8dc,0xb8dfb8de,0xb8e0b8e0,0xb8e2b8e1,0xb8e4b8e3,0xb8e6b8e5,0xb8e8b8e7,0xb8eab8e9,0xb8ecb8eb,0xb8eeb8ed,0xb8efb8ef,
0xb8f3b8f1,0xb8f6b8f5,0xb8fab8f8,0xb8feb8fc,0xb901b900,0xb903b902,0xb905b904,0xb906b906,0xb908b907,0xb90ab909,0xb90cb90b,0xb90eb90d,0xb910b90f,0xb912b911,0xb914b913,0xb915b915,
0xb917b916,0xb919b918,0xb91bb91a,0xb91db91c,0xb91fb91e,0xb921b920,0xb923b922,0xb924b924,0xb926b925,0xb928b927,0xb92ab929,0xb92cb92b,0xb92eb92d,0xb930b92f,0xb932b931,0xb933b933,
0xb935b934,0xb937b936,0xb939b938,0xb93bb93a,0xb93db93c,0xb93fb93e,0xb941b940,0xb942b941,0xb944b943,0xb946b945,0xb948b947,0xb94ab949,0xb94cb94b,0xb94eb94d,0xb950b94f,0xb951b950,
0xb953b952,0xb955b954,0xb957b956,0xb959b958,0xb95bb95a,0xb95db95c,0xb95fb95e,0xb960b95f,0xb962b961,0xb964b963,0xb966b965,0xb968b967,0xb96ab969,0xb96cb96b,0xb96eb96d,0xb96fb96e,
0xb973b971,0xb976b975,0xb97ab978,0xb97eb97c,0xb981b980,0xb983b982,0xb985b984,0xb986b986,0xb988b987,0xb98ab989,0xb98cb98b,0xb98eb98d,0xb990b98f,0xb992b991,0xb994b993,0xb995b994,
0xb997b996,0xb999b998,0xb99bb99a,0xb99db99c,0xb99fb99e,0xb9a1b9a0,0xb9a3b9a2,0xb9a4b9a3,0xb9a6b9a5,0xb9a8b9a7,0xb9aab9a9,0xb9acb9ab,0xb9aeb9ad,0xb9b0b9af,0xb9b2b9b1,0xb9b3b9b2,
0xb9b5b9b4,0xb9b7b9b6,0xb9b9b9b8,0xb9bbb9ba,0xb9bdb9bc,0xb9bfb9be,0xb9c1b9c0,0xb9c2b9c1,0xb9c4b9c3,0xb9c6b9c5,0xb9c8b9c7,0xb9cab9c9,0xb9ccb9cb,0xb9ceb9cd,0xb9cfb9cf,0xb9d1b9d0,
0xb9d3b9d2,0xb9d5b9d4,0xb9d7b9d6,0xb9d9b9d8,0xb9dbb9da,0xb9ddb9dc,0xb9deb9de,0xb9e0b9df,0xb9e2b9e1,0xb9e4b9e3,0xb9e6b9e5,0xb9e8b9e7,0xb9eab9e9,0xb9ecb9eb,0xb9edb9ec,0xb9efb9ee,
0xb9f3b9f1,0xb9f6b9f4,0xb9fab9f8,0xb9feb9fc,0xba01ba00,0xba03ba02,0xba05ba04,0xba06ba05,0xba08ba07,0xba0aba09,0xba0cba0b,0xba0eba0d,0xba10ba0f,0xba12ba11,0xba13ba13,0xba15ba14,
0xba17ba16,0xba19ba18,0xba1bba1a,0xba1dba1c,0xba1fba1e,0xba21ba20,0xba22ba22,0xba24ba23,0xba26ba25,0xba28ba27,0xba2aba29,0xba2cba2b,0xba2eba2d,0xba30ba2f,0xba31ba30,0xba33ba32,
0xba35ba34,0xba37ba36,0xba39ba38,0xba3bba3a,0xba3dba3c,0xba3fba3e,0xba40ba3f,0xba42ba41,0xba44ba43,0xba46ba45,0xba48ba47,0xba4aba49,0xba4cba4b,0xba4dba4d,0xba4fba4e,0xba51ba50,
0xba53ba52,0xba55ba54,0xba57ba56,0xba59ba58,0xba5bba5a,0xba5cba5b,0xba5eba5d,0xba60ba5f,0xba62ba61,0xba64ba63,0xba66ba65,0xba68ba67,0xba6aba69,0xba6bba6a,0xba6dba6c,0xba6fba6e,
0xba72ba71,0xba76ba74,0xba7aba78,0xba7eba7c,0xba81ba7f,0xba83ba82,0xba84ba83,0xba86ba85,0xba88ba87,0xba8aba89,0xba8cba8b,0xba8eba8d,0xba90ba8f,0xba91ba91,0xba93ba92,0xba95ba94,
0xba97ba96,0xba99ba98,0xba9bba9a,0xba9dba9c,0xba9fba9e,0xbaa0ba9f,0xbaa2baa1,0xbaa4baa3,0xbaa6baa5,0xbaa8baa7,0xbaaabaa9,0xbaacbaab,0xbaadbaad,0xbaafbaae,0xbab1bab0,0xbab3bab2,
0xbab5bab4,0xbab7bab6,0xbab9bab8,0xbabbbaba,0xbabcbabb,0xbabebabd,0xbac0babf,0xbac2bac1,0xbac4bac3,0xbac6bac5,0xbac8bac7,0xbac9bac9,0xbacbbaca,0xbacdbacc,0xbacfbace,0xbad1bad0,
0xbad3bad2,0xbad5bad4,0xbad7bad6,0xbad8bad7,0xbadabad9,0xbadcbadb,0xbadebadd,0xbae0badf,0xbae2bae1,0xbae4bae3,0xbae5bae4,0xbae7bae6,0xbae9bae8,0xbaebbaea,0xbaedbaec,0xbaefbaee,
0xbaf2baf0,0xbaf6baf4,0xbaf9baf8,0xbafdbafb,0xbb00baff,0xbb02bb01,0xbb04bb03,0xbb06bb05,0xbb08bb07,0xbb0abb09,0xbb0cbb0b,0xbb0dbb0d,0xbb0fbb0e,0xbb11bb10,0xbb13bb12,0xbb15bb14,
0xbb17bb16,0xbb19bb18,0xbb1bbb1a,0xbb1cbb1b,0xbb1ebb1d,0xbb20bb1f,0xbb22bb21,0xbb24bb23,0xbb26bb25,0xbb28bb27,0xbb29bb28,0xbb2bbb2a,0xbb2dbb2c,0xbb2fbb2e,0xbb31bb30,0xbb33bb32,
0xbb35bb34,0xbb36bb35,0xbb38bb37,0xbb3abb39,0xbb3cbb3b,0xbb3ebb3d,0xbb40bb3f,0xbb42bb41,0xbb43bb42,0xbb45bb44,0xbb47bb46,0xbb49bb48,0xbb4bbb4a,0xbb4dbb4c,0xbb4fbb4e,0xbb50bb4f,
0xbb52bb51,0xbb54bb53,0xbb56bb55,0xbb58bb57,0xbb5abb59,0xbb5bbb5b,0xbb5dbb5c,0xbb5fbb5e,0xbb61bb60,0xbb63bb62,0xbb65bb64,0xbb67bb66,0xbb68bb68,0xbb6abb69,0xbb6cbb6b,0xbb6ebb6d,
0xbb71bb6f,0xbb75bb73,0xbb79bb77,0xbb7cbb7a,0xbb80bb7e,0xbb82bb81,0xbb84bb83,0xbb86bb85,0xbb87bb86,0xbb89bb88,0xbb8bbb8a,0xbb8dbb8c,0xbb8fbb8e,0xbb91bb90,0xbb92bb92,0xbb94bb93,
0xbb96bb95,0xbb98bb97,0xbb9abb99,0xbb9cbb9b,0xbb9ebb9d,0xbb9fbb9e,0xbba1bba0,0xbba3bba2,0xbba5bba4,0xbba7bba6,0xbba9bba8,0xbbaabbaa,0xbbacbbab,0xbbaebbad,0xbbb0bbaf,0xbbb2bbb1,
0xbbb4bbb3,0xbbb6bbb5,0xbbb7bbb6,0xbbb9bbb8,0xbbbbbbba,0xbbbdbbbc,0xbbbfbbbe,0xbbc1bbc0,0xbbc2bbc1,0xbbc4bbc3,0xbbc6bbc5,0xbbc8bbc7,0xbbcabbc9,0xbbccbbcb,0xbbcdbbcc,0xbbcfbbce,
0xbbd1bbd0,0xbbd3bbd2,0xbbd5bbd4,0xbbd7bbd6,0xbbd8bbd7,0xbbdabbd9,0xbbdcbbdb,0xbbdebbdd,0xbbe0bbdf,0xbbe2bbe1,0xbbe3bbe2,0xbbe5bbe4,0xbbe7bbe6,0xbbe9bbe8,0xbbebbbea,0xbbedbbec,
0xbbf0bbee,0xbbf3bbf2,0xbbf7bbf5,0xbbfbbbf9,0xbbfebbfc,0xbc01bc00,0xbc03bc02,0xbc05bc04,0xbc06bc06,0xbc08bc07,0xbc0abc09,0xbc0cbc0b,0xbc0ebc0d,0xbc10bc0f,0xbc11bc10,0xbc13bc12,
0xbc15bc14,0xbc17bc16,0xbc19bc18,0xbc1abc1a,0xbc1cbc1b,0xbc1ebc1d,0xbc20bc1f,0xbc22bc21,0xbc24bc23,0xbc25bc24,0xbc27bc26,0xbc29bc28,0xbc2bbc2a,0xbc2dbc2c,0xbc2ebc2d,0xbc30bc2f,
0xbc32bc31,0xbc34bc33,0xbc36bc35,0xbc37bc36,0xbc39bc38,0xbc3bbc3a,0xbc3dbc3c,0xbc3fbc3e,0xbc40bc3f,0xbc42bc41,0xbc44bc43,0xbc46bc45,0xbc48bc47,0xbc49bc48,0xbc4bbc4a,0xbc4dbc4c,
0xbc4fbc4e,0xbc51bc50,0xbc52bc51,0xbc54bc53,0xbc56bc55,0xbc58bc57,0xbc59bc59,0xbc5bbc5a,0xbc5dbc5c,0xbc5fbc5e,0xbc61bc60,0xbc62bc62,0xbc64bc63,0xbc66bc65,0xbc68bc67,0xbc6abc69,
0xbc6dbc6b,0xbc70bc6e,0xbc74bc72,0xbc77bc76,0xbc7bbc79,0xbc7ebc7d,0xbc81bc80,0xbc83bc82,0xbc85bc84,0xbc86bc85,0xbc88bc87,0xbc8abc89,0xbc8cbc8b,0xbc8dbc8c,0xbc8fbc8e,0xbc91bc90,
0xbc93bc92,0xbc94bc93,0xbc96bc95,0xbc98bc97,0xbc9abc99,0xbc9bbc9b,0xbc9dbc9c,0xbc9fbc9e,0xbca1bca0,0xbca2bca1,0xbca4bca3,0xbca6bca5,0xbca8bca7,0xbca9bca8,0xbcabbcaa,0xbcadbcac,
0xbcafbcae,0xbcb0bcaf,0xbcb2bcb1,0xbcb4bcb3,0xbcb5bcb5,0xbcb7bcb6,0xbcb9bcb8,0xbcbbbcba,0xbcbcbcbc,0xbcbebcbd,0xbcc0bcbf,0xbcc2bcc1,0xbcc3bcc2,0xbcc5bcc4,0xbcc7bcc6,0xbcc8bcc8,
0xbccabcc9,0xbcccbccb,0xbccebccd,0xbccfbcce,0xbcd1bcd0,0xbcd3bcd2,0xbcd4bcd3,0xbcd6bcd5,0xbcd8bcd7,0xbcd9bcd9,0xbcdbbcda,0xbcddbcdc,0xbcdfbcde,0xbce0bcdf,0xbce2bce1,0xbce4bce3,
0xbce7bce5,0xbceabce8,0xbcedbcec,0xbcf1bcef,0xbcf4bcf2,0xbcf7bcf6,0xbcfbbcf9,0xbcfebcfc,0xbd01bd00,0xbd02bd01,0xbd04bd03,0xbd06bd05,0xbd07bd06,0xbd09bd08,0xbd0bbd0a,0xbd0cbd0b,
0xbd0ebd0d,0xbd10bd0f,0xbd11bd10,0xbd13bd12,0xbd14bd14,0xbd16bd15,0xbd18bd17,0xbd19bd18,0xbd1bbd1a,0xbd1dbd1c,0xbd1ebd1d,0xbd20bd1f,0xbd21bd21,0xbd23bd22,0xbd25bd24,0xbd26bd25,
0xbd28bd27,0xbd29bd29,0xbd2bbd2a,0xbd2cbd2c,0xbd2ebd2d,0xbd30bd2f,0xbd31bd30,0xbd33bd32,0xbd34bd34,0xbd36bd35,0xbd38bd37,0xbd39bd38,0xbd3bbd3a,0xbd3cbd3b,0xbd3ebd3d,0xbd3fbd3f,
0xbd41bd40,0xbd42bd42,0xbd44bd43,0xbd45bd45,0xbd47bd46,0xbd49bd48,0xbd4abd49,0xbd4cbd4b,0xbd4dbd4c,0xbd4fbd4e,0xbd50bd4f,0xbd52bd51,0xbd53bd52,0xbd55bd54,0xbd56bd55,0xbd58bd57,
0xbd5abd59,0xbd5dbd5c,0xbd60bd5f,0xbd63bd62,0xbd66bd65,0xbd69bd68,0xbd6cbd6b,0xbd6fbd6e,0xbd72bd70,0xbd75bd73,0xbd78bd76,0xbd7bbd79,0xbd7dbd7c,0xbd80bd7f,0xbd82bd81,0xbd83bd82,
0xbd84bd84,0xbd86bd85,0xbd87bd87,0xbd89bd88,0xbd8abd89,0xbd8bbd8b,0xbd8dbd8c,0xbd8ebd8d,0xbd8fbd8f,0xbd91bd90,0xbd92bd92,0xbd94bd93,0xbd95bd94,0xbd96bd96,0xbd98bd97,0xbd99bd98,
0xbd9abd9a,0xbd9cbd9b,0xbd9dbd9c,0xbd9ebd9d,0xbd9fbd9f,0xbda1bda0,0xbda2bda1,0xbda3bda3,0xbda5bda4,0xbda6bda5,0xbda7bda6,0xbda8bda8,0xbdaabda9,0xbdabbdaa,0xbdacbdab,0xbdadbdad,
0xbdafbdae,0xbdb0bdaf,0xbdb1bdb0,0xbdb2bdb2,0xbdb3bdb3,0xbdb5bdb4,0xbdb6bdb5,0xbdb7bdb6,0xbdb8bdb8,0xbdb9bdb9,0xbdbbbdba,0xbdbcbdbb,0xbdbdbdbc,0xbdbebdbd,0xbdbfbdbf,0xbdc0bdc0,
0xbdc2bdc1,0xbdc5bdc3,0xbdc7bdc6,0xbdc9bdc8,0xbdcbbdca,0xbdcdbdcc,0xbdd0bdce,0xbdd2bdd1,0xbdd4bdd3,0xbdd6bdd5,0xbdd8bdd7,0xbddabdd9,0xbddcbddb,0xbddebddd,0xbde0bddf,0xbde2bde1,
0xbde4bde3,0xbde6bde5,0xbde8bde7,0xbde9bde9,0xbdebbdea,0xbdedbdec,0xbdefbdee,0xbdf1bdf0,0xbdf3bdf2,0xbdf4bdf3,0xbdf6bdf5,0xbdf8bdf7,0xbdf9bdf9,0xbdfbbdfa,0xbdfdbdfc,0xbdfebdfe,
0xbe00bdff,0xbe01be00,0xbe02be01,0xbe02be02,0xbe03be03,0xbe04be03,0xbe05be04,0xbe05be05,0xbe06be06,0xbe07be06,0xbe07be07,0xbe08be08,0xbe09be08,0xbe09be09,0xbe0abe0a,0xbe0bbe0a,
0xbe0bbe0b,0xbe0cbe0c,0xbe0dbe0c,0xbe0dbe0d,0xbe0ebe0e,0xbe0fbe0e,0xbe0fbe0f,0xbe10be0f,0xbe10be10,0xbe11be11,0xbe11be11,0xbe12be12,0xbe12be12,0xbe13be13,0xbe13be13,0xbe14be14,
0xbe15be14,0xbe16be15,0xbe17be16,0xbe18be17,0xbe18be18,0xbe19be19,0xbe1abe1a,0xbe1bbe1a,0xbe1bbe1b,0xbe1cbe1c,0xbe1dbe1c,0xbe1dbe1d,0xbe1ebe1e,0xbe1ebe1e,0xbe1fbe1f,0xbe1fbe1f,
0xbe20be20,0xbe20be20,0xbe21be21,0xbe21be21,0xbe21be21,0xbe22be22,0xbe22be22,0xbe22be22,0xbe22be22,0xbe23be23,0xbe23be23,0xbe23be23,0xbe23be23,0xbe23be23,0xbe23be23,0xbe23be23,
0xbe23be23,0xbe23be23,0xbe23be23,0xbe23be23,0xbe23be23,0xbe23be23,0xbe23be23,0xbe23be23,0xbe22be22,0xbe22be22,0xbe22be22,0xbe22be22,0xbe21be22,0xbe21be21,0xbe21be21,0xbe20be21,
0xbe20be20,0xbe20be20,0xbe1fbe1f,0xbe1fbe1f,0xbe1ebe1f,0xbe1ebe1e,0xbe1ebe1e,0xbe1dbe1d,0xbe1dbe1d,0xbe1cbe1c,0xbe1bbe1c,0xbe1bbe1b,0xbe1abe1b,0xbe1abe1a,0xbe19be19,0xbe19be19,
0xbe17be18,0xbe16be17,0xbe15be15,0xbe13be14,0xbe12be13,0xbe10be11,0xbe0fbe10,0xbe0dbe0e,0xbe0cbe0c,0xbe0abe0b,0xbe08be09,0xbe07be07,0xbe05be06,0xbe03be04,0xbe01be02,0xbdffbe00,
0xbdfbbdfd,0xbdf7bdf9,0xbdf4bdf6,0xbdf0bdf2,0xbdecbdee,0xbde8bdea,0xbde4bde6,0xbde1bde3,0xbdddbddf,0xbdd9bddb,0xbdd5bdd7,0xbdd1bdd3,0xbdcdbdcf,0xbdc9bdcb,0xbdc6bdc8,0xbdc2bdc4,
0xbdbebdc0,0xbdbabdbc,0xbdb6bdb8,0xbdb3bdb4,0xbdafbdb1,0xbdabbdad,0xbda7bda9,0xbda4bda6,0xbda0bda2,0xbd9cbd9e,0xbd99bd9b,0xbd95bd97,0xbd92bd94,0xbd8ebd90,0xbd8bbd8d,0xbd88bd89,
0xbd84bd86,0xbd81bd83,0xbd7bbd7f,0xbd75bd78,0xbd6fbd72,0xbd69bd6c,0xbd62bd65,0xbd5cbd5f,0xbd56bd59,0xbd51bd53,0xbd4bbd4e,0xbd45bd48,0xbd40bd42,0xbd3abd3d,0xbd35bd38,0xbd30bd32,
0xbd27bd2c,0xbd1dbd22,0xbd13bd18,0xbd0abd0f,0xbd02bd06,0xbcf3bcfb,0xbce3bceb,0xbcd4bcdc,0xbcc6bccd,0xbcb9bcbf,0xbcacbcb2,0xbca0bca6,0xbc95bc9a,0xbc8abc8f,0xbc7fbc85,0xbc6cbc76,
0xbc5abc63,0xbc49bc52,0xbc3abc41,0xbc2bbc32,0xbc1dbc24,0xbc10bc17,0xbc04bc0a,0xbbf3bbfd,0xbbdebbe8,0xbbcbbbd4,0xbbb9bbc2,0xbba8bbb1,0xbb99bba1,0xbb8bbb92,0xbb7dbb85,0xbb65bb71,
0xbb4fbb5a,0xbb3bbb45,0xbb29bb32,0xbb18bb21,0xbb09bb11,0xbaf6bb02,0xbaddbae9,0xbac6bad1,0xbab1babb,0xba9ebaa8,0xba8dba96,0xba7cba85,0xba60ba6d,0xba47ba53,0xba30ba3b,0xba1cba26,
0xba0aba13,0xb9f4ba02,0xb9d7b9e5,0xb9bdb9c9,0xb9a6b9b1,0xb992b99b,0xb97fb988,0xb95fb96e,0xb943b950,0xb92ab936,0xb913b91e,0xb900b909,0xb8deb8ef,0xb8c0b8cf,0xb8a6b8b3,0xb88fb89a,
0xb85cb880,0xb822b83d,0xb7edb80b,0xb7acb7ca,0xb777b792,0xb731b751,0xb6fbb715,0xb6b1b6d3,0xb677b694,0xb62bb64e,0xb5ebb60e,0xb5a0b5c2,0xb559b584,0xb512b532,0xb4c2b4ee,0xb480b49e,
0xb428b450,0xb3dab407,0xb38cb3af,0xb333b360,0xb2e2b30e,0xb28db2b3,0xb230b25f,0xb1d8b20a,0xb184b1a9,0xb11fb14d,0xb0beb0f6,0xb061b092,0xb004b02c,0xaf99afc9,0xaf30af69,0xaec9af05,
0xae62ae97,0xadfcae29,0xad8badbc,0xad18ad4e,0xaca5ace0,0xac30ac71,0xabbaac00,0xab43ab87,0xaacaab0d,0xaa4faa91,0xa9d2aa13,0xa952a995,0xa8d0a914,0xa84ca892,0xa7c5a80e,0xa73ca788,
0xa6b2a702,0xa626a674,0xa59aa5e2,0xa50ca550,0xa47ca4bc,0xa3e1a429,0xa345a395,0xa2aba302,0xa213a261,0xa179a1c0,0xa0d0a121,0xa02ca086,0x9f8c9fdb,0x9ee19f31,0x9e329e8e,0x9d8b9ddf,
0x9c519d09,0x9aeb9b9e,0x99799a2c,0x97fa98b2,0x966d972e,0x94d495a0,0x9333940b,0x918f9264,0x8fd690b0,0x8e178f00,0x8c488d2f,0x8a798b61,0x88918987,0x80008799,0x80008000,0x80008000,
0x80008000,0x80008000,0x80008000,0x80008000,0x80008000,0x80008000,0x80008000,0x80008000,0x80008000,0x80008000,0x80008000,0x80008000,0x80008000,0x80008000,0x80008000,0x80008000,
0x80008000,0x80008000,0x80008000,0x80008000,0x80008000,0x80008000,0x80008000,0x80008000,0x80008000,0x80008000,0x80008000,0x80008000,0x80008000,0x80008000,0x80008000,0x80008000,
0x80008000,0x80008000,0x80008000,0x80008000,0x80008000,0x80008000,0x80008000,0x80008000,0x80008000,0x80008000,0x80008000,0x80008000,0x80008000,0x80008000,0x80008000,0x80008000,
0x80008000,0x80008000,0x80008000,0x80008000,0x80008000,0x80008000,0x80008000,0x80008000,0x80008000,0x80008000,0x80008000,0x80008000,0x80008000,0x80008000,0x80008000,0x80008000,
0x80008000,0x80008000,0x80008000,0x80008000,0x80008000,0x80008000,0x80008000,0x80008000,0x80008000,0x80008000,0x80008000,0x80008000,0x80008000,0x80008000,0x80008000,0x80008000,
0x80008000,0x80008000,0x80008000,0x80008000,0x80008000,0x80008000,0x80008000,0x80008000,0x80008000,0x80008000,0x80008000,0x80008000,0x80008000,0x80008000,0x80008000,0x80008000,
0x80008000,0x80008000,0x80008000,0x80008000,0x80008000,0x80008000,0x80008000,0x80008000,0x80008000,0x80008000,0x80008000,0x80008000,0x80008000,0x80008000,0x80008000,0x80008000,
0x80008000,0x80008000,0x80008000,0x80008000,0x80008000,0x80008000,0x80008000,0x80008000,0x80008000,0x80008000,0x80008000,0x80008000,0x80008000,0x80008000,0x80008000,0x80008000,
0x80008000,0x80008000,0x80008000,0x80008000,0x80008000,0x80008000,0x80008000,0x80008000,0x80008000,0x80008000,0x80008000,0x80008000,0x80008000,0x80008000,0x80008000,0x80008000,
0x80008000,0x80008000,0x80008000,0x80008000,0x80008000,0x80008000,0x80008000,0x80008000,0x80008000,0x80008000,0x80008000,0x80008000,0x80008000,0x80008000,0x80008000,0x80008000,
0x80008000,0x80008000,0x80008000,0x80008000,0x80008000,0x80008000,0x80008000,0x80008000,0x80008000,0x80008000,0x80008000,0x80008000,0x80008000,0x80008000,0x80008000,0x80008000,
0x80008000,0x80008000,0x80008000,0x80008000,0x80008000,0x80008000,0x80008000,0x80008000,0x80008000,0x80008000,0x80008000,0x80008000,0x80008000,0x80008000,0x80008000,0x80008000,
0x80008000,0x80008000,0x80008000,0x80008000,0x80008000,0x80008000,0x80008000,0x80008000,0x80008000,0x80008000,0x80008000,0x80008000,0x80008000,0x80008000,0x80008000,0x80008000,
0x80008000,0x80008000,0x80008000,0x80008000,0x80008000,0x80008000,0x80008000,0x80008000,0x80008000,0x80008000,0x80008000,0x80008000,0x80008000,0x80008000,0x80008000,0x80008000,
0x80008000,0x80008000,0x80008000,0x80008000,0x80008000,0x80008000,0x80008000,0x80008000,0x80008000,0x80008000,0x80008000,0x80008000,0x80008000,0x80008000,0x80008000,0x80008000,
0x80008000,0x80008000,0x80008000,0x80008000,0x80008000,0x80008000,0x80008000,0x80008000,0x80008000,0x80008000,0x80008000,0x80008000,0x80008000,0x80008000,0x80008000,0x80008000,
0x80008000,0x80008000,0x80008000,0x80008000,0x80008000,0x80008000,0x80008000,0x80008000,0x80008000,0x80008000,0x80008000,0x80008000,0x80008000,0x80008000,0x80008000,0x80008000,
0x80008000,0x80008000,0x80008000,0x80008000,0x80008000,0x80008000,0x80008000,0x80008000,0x80008000,0x80008000,0x80008000,0x80008000,0x80008000,0x80008000,0x80008000,0x80008000,
0x80008000,0x80008000,0x80008000,0x80008000,0x80008000,0x80008000,0x80008000,0x80008000,0x80008000,0x80008000,0x80008000,0x80008000,0x80008000,0x80008000,0x80008000,0x80008000,
0x80008000,0x80008000,0x80008000,0x80008000,0x80008000,0x80008000,0x80008000,0x80008000,0x80008000,0x80008000,0x80008000,0x80008000,0x80008000,0x80008000,0x80008000,0x80008000,
0x80008000,0x80008000,0x80008000,0x80008000,0x80008000,0x80008000,0x80008000,0x80008000,0x80008000,0x80008000,0x80008000,0x80008000,0x80008000,0x80008000,0x80008000,0x80008000,
0x80008000,0x80008000,0x80008000,0x80008000,0x80008000,0x80008000,0x80008000,0x80008000,0x80008000,0x80008000,0x80008000,0x80008000,0x80008000,0x80008000,0x80008000,0x80008000,
0x80008000,0x80008000,0x80008000,0x80008000,0x80008000,0x80008000,0x80008000,0x80008000,0x80008000,0x80008000,0x80008000,0x80008000,0x80008000,0x80008000,0x80008000,0x80008000,
};
__global__ __launch_bounds__(512, 2) void mixer_token_mlp(MxArgs a) {
  unsigned tid = threadIdx.x;
  unsigned bid = blockIdx.x;
  const void* tab = (const void*)kGeluTab;
  const void* ka = (const void*)__builtin_amdgcn_kernarg_segment_ptr();
  (void)a;

  asm volatile(
    "s_load_dwordx8 s[4:11], %0, 0x0\n"
    "s_load_dwordx4 s[12:15], %0, 0x20\n"
    "s_load_dwordx2 s[16:17], %0, 0x30\n"
    "s_getpc_b64 s[98:99]\n"
    "v_lshlrev_b32 v236, 7, %2\n"
    "v_min_u32 v236, 15000, v236\n"
    "global_load_dword v237, v236, s[98:99]\n"
    "v_and_b32 v1, 63, %2\n"
    "v_lshrrev_b32 v13, 6, %2\n"
    "s_mov_b32 s38, 1179648\n"
    "s_mov_b32 s39, 0x20000\n"
    "v_readfirstlane_b32 s68, v13\n"
    "s_and_b32 s86, %1, 7\n"
    "s_lshr_b32 s87, %1, 3\n"
    "s_lshr_b32 s88, s87, 3\n"
    "s_lshl_b32 s70, s86, 2\n"
    "s_add_u32 s70, s70, s88\n"
    "s_lshr_b32 s88, s87, 2\n"
    "s_and_b32 s71, s88, 1\n"
    "s_lshl_b32 s73, s86, 3\n"
    "s_add_u32 s73, s73, s88\n"
    "s_and_b32 s69, s87, 3\n"
    "s_mul_i32 s90, s70, 36864\n"
    "s_mul_i32 s86, s71, 576\n"
    "s_add_u32 s86, s86, s90\n"
    "s_lshl_b32 s87, s69, 7\n"
    "s_add_u32 s72, s86, s87\n"
    "s_add_u32 s91, s86, 512\n"
    "v_lshrrev_b32 v136, 4, v1\n"
    "s_lshl_b32 s88, s68, 2\n"
    "v_add_u32 v136, s88, v136\n"
    "v_and_b32 v137, 15, v1\n"
    "v_mul_u32_u24 v138, 1152, v136\n"
    "v_lshl_add_u32 v12, v137, 4, v138\n"
    "s_waitcnt lgkmcnt(0)\n"
    "s_mov_b64 s[36:37], s[4:5]\n"
    "s_and_b32 s37, s37, 0xffff\n"
    "buffer_load_dwordx4 v[104:107], v12, s[36:39], s90 offen offset:0\n"
    "buffer_load_dwordx4 v[108:111], v12, s[36:39], s90 offen offset:256\n"
    "buffer_load_dwordx4 v[112:115], v12, s[36:39], s90 offen offset:512\n"
    "buffer_load_dwordx4 v[116:119], v12, s[36:39], s90 offen offset:768\n"
    "v_lshl_add_u32 v13, v137, 3, v138\n"
    "buffer_load_dwordx2 v[120:121], v13, s[36:39], s90 offen offset:1024\n"
    "v_and_b32 v139, 3, v137\n"
    "v_lshl_add_u32 v15, v139, 4, v138\n"
    "v_add_u32 v15, s91, v15\n"
    "v_add_u32 v12, s72, v12\n"
    "v_cmp_gt_u32 vcc, 8, v137\n"
    "s_nop 1\n"
    "v_cndmask_b32 v12, v15, v12, vcc\n"
    "buffer_load_dwordx4 v[122:125], v12, s[36:39], 0 offen\n"
    "s_mov_b64 s[40:41], s[6:7]\n"
    "s_and_b32 s41, s41, 0xffff\n"
    "s_mov_b32 s42, 294912\n"
    "s_mov_b32 s43, 0x20000\n"
    "s_mov_b64 s[48:49], s[8:9]\n"
    "s_and_b32 s49, s49, 0xffff\n"
    "s_mov_b32 s50, 9216\n"
    "s_mov_b32 s51, 0x20000\n"
    "s_mov_b64 s[64:65], %3\n"
    "s_and_b32 s65, s65, 0xffff\n"
    "s_mov_b32 s66, 16384\n"
    "s_mov_b32 s67, 0x20000\n"
    "s_and_b32 s86, s68, 3\n"
    "s_lshl_b32 s87, s86, 2\n"
    "s_add_u32 s87, s87, s69\n"
    "s_mov_b32 s88, 0x54205320\n"
    "s_mov_b32 s89, 0x53106420\n"
    "s_mov_b32 s75, 0xa96a9aa6\n"
    "s_mov_b32 s84, 7\n"
    "s_mov_b32 s92, 44\n"
    "s_cmp_lt_u32 s68, 4\n"
    "s_cselect_b32 s88, 0x85209630, s88\n"
    "s_cselect_b32 s89, 0x86308530, s89\n"
    "s_cselect_b32 s75, 0xeffbfebf, s75\n"
    "s_cselect_b32 s84, 11, s84\n"
    "s_cselect_b32 s92, 0, s92\n"
    "s_mul_i32 s95, s84, s86\n"
    "s_add_u32 s92, s92, s95\n"
    "s_cmp_ge_u32 s86, 2\n"
    "s_cselect_b32 s88, s89, s88\n"
    "s_and_b32 s89, s87, 7\n"
    "s_lshl_b32 s89, s89, 2\n"
    "s_lshr_b32 s74, s88, s89\n"
    "s_and_b32 s74, s74, 15\n"
    "s_lshl_b32 s89, s87, 1\n"
    "s_lshr_b32 s75, s75, s89\n"
    "s_and_b32 s75, s75, 3\n"
    "s_mul_i32 s95, s84, 128\n"
    "s_sub_u32 s84, s84, s75\n"
    "s_lshl_b32 s81, s86, 13\n"
    "s_lshr_b32 s87, s68, 2\n"
    "s_lshl_b32 s87, s87, 16\n"
    "s_add_u32 s81, s81, s87\n"
    "s_add_u32 s81, s81, 32768\n"
    "s_add_u32 s86, s92, s74\n"
    "s_lshl_b32 s76, s86, 7\n"
    "s_add_u32 s77, s76, 145408\n"
    "s_lshl_b32 s78, s86, 12\n"
    "s_add_u32 s79, s78, 2048\n"
    "s_lshl_b32 s85, s74, 7\n"
    "v_lshrrev_b32 v13, 3, v1\n"
    "v_bfe_u32 v14, v1, 4, 1\n"
    "v_lshlrev_b32 v14, 2, v14\n"
    "v_lshrrev_b32 v15, 5, v1\n"
    "v_or_b32 v14, v14, v15\n"
    "v_and_b32 v15, 7, v1\n"
    "v_xor_b32 v15, v15, v14\n"
    "v_mul_u32_u24 v13, 9216, v13\n"
    "v_lshl_add_u32 v2, v15, 4, v13\n"
    "v_xor_b32 v15, 2, v15\n"
    "v_lshl_add_u32 v3, v15, 4, v13\n"
    "v_add_u32 v3, 72704, v3\n"
    "v_lshlrev_b32 v4, 4, v1\n"
    "v_lshlrev_b32 v239, 2, v1\n"
    "s_lshl_b32 s98, s92, 7\n"
    "s_sub_u32 s99, s95, 1024\n"
    "v_lshlrev_b32 v75, 3, v137\n"
    "v_cvt_f32_u32 v75, v75\n"
    "v_mov_b32 v76, 0x3b6ff558\n"
    "v_fmaak_f32 v40, v76, v75, 0x3ef0e54d\n"
    "v_fmaak_f32 v41, v76, v75, 0x3ef2c538\n"
    "v_fmaak_f32 v42, v76, v75, 0x3ef4a523\n"
    "v_fmaak_f32 v43, v76, v75, 0x3ef6850d\n"
    "v_fmaak_f32 v44, v76, v75, 0x3ef864f8\n"
    "v_fmaak_f32 v45, v76, v75, 0x3efa44e3\n"
    "v_fmaak_f32 v46, v76, v75, 0x3efc24cd\n"
    "v_fmaak_f32 v47, v76, v75, 0x3efe04b8\n"
    "v_cvt_pk_bf16_f32 v48, v40, v41\n"
    "v_cvt_pk_bf16_f32 v49, v42, v43\n"
    "v_cvt_pk_bf16_f32 v50, v44, v45\n"
    "v_cvt_pk_bf16_f32 v51, v46, v47\n"
    "v_lshrrev_b32 v76, 4, v1\n"
    "v_mul_u32_u24 v76, 0x800080, v76\n"
    "s_lshl_b32 s86, s68, 2\n"
    "s_sub_u32 s87, 63, s86\n"
    "s_mul_i32 s87, s87, 0x800080\n"
    "v_subrev_u32 v76, s87, v76\n"
    "v_add_u32 v48, v48, v76\n"
    "v_add_u32 v49, v49, v76\n"
    "v_add_u32 v50, v50, v76\n"
    "v_add_u32 v51, v51, v76\n"
    "s_lshl_b32 s87, s68, 10\n"
    "v_lshl_add_u32 v72, v1, 4, s87\n"
    "ds_write_b128 v72, v[48:51]\n"
    "v_add_u32 v74, 0x10000, v72\n"
    "v_or_b32 v56, 0x80008000, v48\n"
    "v_or_b32 v57, 0x80008000, v49\n"
    "v_or_b32 v58, 0x80008000, v50\n"
    "v_or_b32 v59, 0x80008000, v51\n"
    "ds_write_b128 v74, v[56:59]\n"
    "v_add_u32 v60, 0x24802480, v48\n"
    "v_add_u32 v61, 0x24802480, v49\n"
    "v_add_u32 v62, 0x24802480, v50\n"
    "v_add_u32 v63, 0x24802480, v51\n"
    "ds_write_b128 v72, v[60:63] offset:18432\n"
    "v_add_u32 v64, 0x30803080, v48\n"
    "v_add_u32 v65, 0x30803080, v49\n"
    "v_add_u32 v66, 0x30803080, v50\n"
    "v_add_u32 v67, 0x30803080, v51\n"
    "ds_write_b128 v72, v[64:67] offset:24576\n"
    "v_mov_b32 v68, 0x80008000\n"
    "v_mov_b32 v69, 0x80008000\n"
    "v_mov_b32 v70, 0x80008000\n"
    "v_mov_b32 v71, 0x80008000\n"
    "ds_write_b128 v74, v[68:71] offset:18432\n"
    "ds_write_b128 v74, v[68:71] offset:24576\n"
    "s_and_b32 s87, s68, 1\n"
    "s_lshl_b32 s88, s87, 2\n"
    "s_add_u32 s88, s88, 32\n"
    "s_sub_u32 s88, s88, s86\n"
    "s_mul_i32 s88, s88, 0x800080\n"
    "s_add_u32 s87, s87, 8\n"
    "s_lshl_b32 s87, s87, 10\n"
    "s_add_u32 s89, s88, 0x80008000\n"
    "s_add_u32 s86, s87, 0x10000\n"
    "s_bitcmp1_b32 s68, 1\n"
    "s_cselect_b32 s88, s89, s88\n"
    "s_cselect_b32 s87, s86, s87\n"
    "s_mov_b32 m0, s81\n"
    "s_nop 0\n"
    "buffer_load_dwordx4 v2, s[40:43], s76 offen lds\n"
    "v_lshl_add_u32 v73, v1, 4, s87\n"
    "v_add_u32 v52, s88, v48\n"
    "v_add_u32 v53, s88, v49\n"
    "v_add_u32 v54, s88, v50\n"
    "v_add_u32 v55, s88, v51\n"
    "ds_write_b128 v73, v[52:55]\n"
    "v_mov_b64 v[88:89], 0\n"
    "v_mov_b64 v[90:91], 0\n"
    "s_cmp_lg_u32 s68, 0\n"
    "s_cbranch_scc1 .Lmx_nz\n"
    "v_lshlrev_b32 v75, 2, v1\n"
    "v_add_u32 v76, 0x10000, v75\n"
    "ds_write_b32 v75, v88\n"
    "ds_write_b32 v76, v88\n"
    ".Lmx_nz:\n"
    "v_lshrrev_b32 v140, 2, v137\n"
    "v_min_u32 v140, 2, v140\n"
    "v_lshlrev_b32 v140, 11, v140\n"
    "buffer_load_dwordx4 v3, s[40:43], s76 offen offset:1024 lds\n"
    "v_lshl_add_u32 v140, v136, 6, v140\n"
    "v_lshl_add_u32 v140, v139, 4, v140\n"
    "v_add_u32 v140, 140288, v140\n"
    "v_bfe_u32 v13, v1, 2, 2\n"
    "v_and_b32 v14, 3, v1\n"
    "v_lshrrev_b32 v144, 5, v1\n"
    "v_lshl_add_u32 v142, v144, 2, v13\n"
    "v_bfe_u32 v15, v1, 4, 1\n"
    "v_lshlrev_b32 v12, 6, v142\n"
    "v_lshl_add_u32 v12, v15, 5, v12\n"
    "v_lshl_add_u32 v12, v14, 3, v12\n"
    "s_add_u32 s86, s81, 4096\n"
    "v_add_u32 v9, s86, v12\n"
    "v_lshl_add_u32 v9, v144, 8, v9\n"
    "v_lshlrev_b32 v15, 1, v15\n"
    "v_lshrrev_b32 v143, 1, v14\n"
    "v_or_b32 v15, v15, v143\n"
    "v_lshrrev_b32 v143, 1, v13\n"
    "v_lshl_or_b32 v143, v143, 2, v144\n"
    "v_xor_b32 v15, v15, v143\n"
    "buffer_load_dwordx4 v2, s[40:43], s77 offen offset:2048 lds\n"
    "v_lshlrev_b32 v142, 7, v142\n"
    "v_and_b32 v14, 1, v14\n"
    "v_lshl_add_u32 v142, v14, 3, v142\n"
    "v_add_u32 v142, s81, v142\n"
    "v_xor_b32 v143, 0, v15\n"
    "v_lshl_add_u32 v5, v143, 4, v142\n"
    "v_xor_b32 v143, 2, v15\n"
    "v_lshl_add_u32 v6, v143, 4, v142\n"
    "v_xor_b32 v143, 4, v15\n"
    "v_lshl_add_u32 v7, v143, 4, v142\n"
    "v_xor_b32 v143, 6, v15\n"
    "v_lshl_add_u32 v8, v143, 4, v142\n"
    "v_and_b32 v15, 31, v1\n"
    "s_add_u32 s86, s98, 131072\n"
    "v_lshl_add_u32 v10, v15, 1, s86\n"
    "v_add_u32 v11, s85, v10\n"
    "v_lshlrev_b32 v15, 2, v1\n"
    "v_add_u32 v15, 162816, v15\n"
    "ds_write_b32 v15, v88\n"
    "s_cmp_lt_u32 s68, 4\n"
    "s_mov_b32 s86, 0x3ef0\n"
    "s_cselect_b32 s86, 0x1f80, s86\n"
    "s_mov_b32 s87, 0xb8ab\n"
    "s_cselect_b32 s87, 0, s87\n"
    "s_mov_b32 s88, 140288\n"
    "s_cselect_b32 s88, 146432, s88\n"
    "buffer_load_dwordx4 v3, s[40:43], s77 offen offset:3072 lds\n"
    "v_mov_b32 v180, s86\n"
    "v_mov_b32 v15, s87\n"
    "v_cmp_lt_u32 vcc, 31, v1\n"
    "s_nop 1\n"
    "v_cndmask_b32 v180, v180, v15, vcc\n"
    "v_add_u32 v12, s88, v12\n"
    "v_mov_b32 v177, 0\n"
    "v_mov_b64 v[178:179], 0\n"
    "v_mov_b32 v181, 0\n"
    "v_mov_b64 v[182:183], 0\n"
    "s_lshl_b32 s34, s74, 7\n"
    "s_add_u32 s34, s34, s98\n"
    "s_add_u32 m0, s34, 131072\n"
    "s_mov_b32 exec_hi, 0\n"
    "buffer_load_dword v239, s[48:51], s34 offen lds\n"
    "s_mov_b32 exec_hi, -1\n"
    "s_mov_b64 s[44:45], s[10:11]\n"
    "s_and_b32 s45, s45, 0xffff\n"
    "s_mov_b32 s46, 294912\n"
    "s_mov_b32 s47, 0x20000\n"
    "s_mov_b64 s[56:57], s[16:17]\n"
    "s_and_b32 s57, s57, 0xffff\n"
    "s_mov_b32 s58, 3145728\n"
    "s_mov_b32 s59, 0x20000\n"
    "s_mov_b64 s[60:61], s[12:13]\n"
    "s_and_b32 s61, s61, 0xffff\n"
    "s_mov_b32 s62, 64\n"
    "s_mov_b32 s63, 0x20000\n"
    "s_mov_b64 s[52:53], s[14:15]\n"
    "s_and_b32 s53, s53, 0xffff\n"
    "s_mov_b32 s54, 1179648\n"
    "s_mov_b32 s55, 0x20000\n"
    "s_add_u32 s94, s69, 3\n"
    "s_and_b32 s94, s94, 3\n"
    "s_or_b32 s94, s94, 4\n"
    "s_mov_b32 s93, 0\n"
    "s_mov_b32 s82, 0x409d30fc\n"
    "s_mov_b32 s83, 0x409d30fc\n"
    "s_mov_b32 s96, 0x3eff4f87\n"
    "s_mov_b32 s97, 0x3eff4f87\n"
    "s_waitcnt lgkmcnt(0)\n"
    "v_mfma_f32_32x32x16_bf16 v[40:55], v[88:91], v[88:91], 0\n"
    "v_mfma_f32_32x32x16_bf16 v[56:71], v[88:91], v[88:91], 0\n"
    "v_mfma_f32_32x32x16_bf16 v[72:87], v[88:91], v[88:91], 0\n"
    "v_mfma_f32_32x32x16_bf16 v[184:199], v[88:91], v[88:91], 0\n"
    "v_mfma_f32_32x32x16_bf16 v[200:215], v[88:91], v[88:91], 0\n"
    "v_mov_b32 v144, 0x3f803f80\n"
    "v_mov_b32 v142, 0\n"
    "v_mov_b32 v143, 0\n"
    "s_waitcnt vmcnt(10)\n"
    "v_dot2_f32_bf16 v142, v104, v144, v142\n"
    "v_dot2_f32_bf16 v143, v104, v104, v143\n"
    "v_dot2_f32_bf16 v142, v105, v144, v142\n"
    "v_dot2_f32_bf16 v143, v105, v105, v143\n"
    "v_dot2_f32_bf16 v142, v106, v144, v142\n"
    "v_dot2_f32_bf16 v143, v106, v106, v143\n"
    "v_dot2_f32_bf16 v142, v107, v144, v142\n"
    "v_dot2_f32_bf16 v143, v107, v107, v143\n"
    "s_waitcnt vmcnt(9)\n"
    "v_dot2_f32_bf16 v142, v108, v144, v142\n"
    "v_dot2_f32_bf16 v143, v108, v108, v143\n"
    "v_dot2_f32_bf16 v142, v109, v144, v142\n"
    "v_dot2_f32_bf16 v143, v109, v109, v143\n"
    "v_dot2_f32_bf16 v142, v110, v144, v142\n"
    "v_dot2_f32_bf16 v143, v110, v110, v143\n"
    "v_dot2_f32_bf16 v142, v111, v144, v142\n"
    "v_dot2_f32_bf16 v143, v111, v111, v143\n"
    "s_waitcnt vmcnt(8)\n"
    "v_dot2_f32_bf16 v142, v112, v144, v142\n"
    "v_dot2_f32_bf16 v143, v112, v112, v143\n"
    "v_dot2_f32_bf16 v142, v113, v144, v142\n"
    "v_dot2_f32_bf16 v143, v113, v113, v143\n"
    "v_dot2_f32_bf16 v142, v114, v144, v142\n"
    "v_dot2_f32_bf16 v143, v114, v114, v143\n"
    "v_dot2_f32_bf16 v142, v115, v144, v142\n"
    "v_dot2_f32_bf16 v143, v115, v115, v143\n"
    "s_waitcnt vmcnt(7)\n"
    "v_dot2_f32_bf16 v142, v116, v144, v142\n"
    "v_dot2_f32_bf16 v143, v116, v116, v143\n"
    "v_dot2_f32_bf16 v142, v117, v144, v142\n"
    "v_dot2_f32_bf16 v143, v117, v117, v143\n"
    "v_dot2_f32_bf16 v142, v118, v144, v142\n"
    "v_dot2_f32_bf16 v143, v118, v118, v143\n"
    "v_dot2_f32_bf16 v142, v119, v144, v142\n"
    "v_dot2_f32_bf16 v143, v119, v119, v143\n"
    "s_waitcnt vmcnt(6)\n"
    "v_dot2_f32_bf16 v142, v120, v144, v142\n"
    "v_dot2_f32_bf16 v143, v120, v120, v143\n"
    "v_dot2_f32_bf16 v142, v121, v144, v142\n"
    "v_dot2_f32_bf16 v143, v121, v121, v143\n"
    "s_lshl_b32 s35, s68, 10\n"
    "s_add_u32 m0, s35, 10240\n"
    "s_nop 0\n"
    "buffer_load_dwordx4 v4, s[64:67], s35 offen lds\n"
    "s_add_u32 m0, s35, 75776\n"
    "s_add_u32 s35, s35, 8192\n"
    "buffer_load_dwordx4 v4, s[64:67], s35 offen lds\n"
    "v_add_f32_dpp v142, v142, v142 row_ror:8 row_mask:0xf bank_mask:0xf\n"
    "v_add_f32_dpp v143, v143, v143 row_ror:8 row_mask:0xf bank_mask:0xf\n"
    "s_nop 0\n"
    "v_add_f32_dpp v142, v142, v142 row_ror:4 row_mask:0xf bank_mask:0xf\n"
    "v_add_f32_dpp v143, v143, v143 row_ror:4 row_mask:0xf bank_mask:0xf\n"
    "s_nop 0\n"
    "v_add_f32_dpp v142, v142, v142 row_ror:2 row_mask:0xf bank_mask:0xf\n"
    "v_add_f32_dpp v143, v143, v143 row_ror:2 row_mask:0xf bank_mask:0xf\n"
    "s_nop 0\n"
    "v_add_f32_dpp v142, v142, v142 row_ror:1 row_mask:0xf bank_mask:0xf\n"
    "v_add_f32_dpp v143, v143, v143 row_ror:1 row_mask:0xf bank_mask:0xf\n"
    "v_mul_f32 v142, 0x3ae38e39, v142\n"
    "v_mul_f32 v143, 0x3ae38e39, v143\n"
    "v_fma_f32 v143, -v142, v142, v143\n"
    "v_max_f32 v143, 0, v143\n"
    "v_add_f32 v143, 0x3727c5ac, v143\n"
    "v_rsq_f32 v145, v143\n"
    "s_nop 0\n"
    "v_mul_f32 v151, 0x1f800000, v145\n"
    "v_mul_f32 v145, 0x3eeff558, v145\n"
    "v_mul_f32 v13, v142, v151\n"
    "v_mul_f32 v146, v142, v145\n"
    "s_waitcnt vmcnt(7)\n"
    "v_lshlrev_b32 v147, 16, v122\n"
    "v_and_b32 v148, 0xffff0000, v122\n"
    "v_fma_f32 v149, v147, v145, -v146\n"
    "v_fma_f32 v150, v148, v145, -v146\n"
    "v_fma_f32 v147, v147, v151, -v13\n"
    "v_fma_f32 v148, v148, v151, -v13\n"
    "v_cvt_pk_bf16_f32 v128, v149, v150\n"
    "v_cvt_pk_bf16_f32 v132, v147, v148\n"
    "v_lshlrev_b32 v147, 16, v123\n"
    "v_and_b32 v148, 0xffff0000, v123\n"
    "v_fma_f32 v149, v147, v145, -v146\n"
    "v_fma_f32 v150, v148, v145, -v146\n"
    "v_fma_f32 v147, v147, v151, -v13\n"
    "v_fma_f32 v148, v148, v151, -v13\n"
    "v_cvt_pk_bf16_f32 v129, v149, v150\n"
    "v_cvt_pk_bf16_f32 v133, v147, v148\n"
    "v_lshlrev_b32 v147, 16, v124\n"
    "v_and_b32 v148, 0xffff0000, v124\n"
    "v_fma_f32 v149, v147, v145, -v146\n"
    "v_fma_f32 v150, v148, v145, -v146\n"
    "v_fma_f32 v147, v147, v151, -v13\n"
    "v_fma_f32 v148, v148, v151, -v13\n"
    "v_cvt_pk_bf16_f32 v130, v149, v150\n"
    "v_cvt_pk_bf16_f32 v134, v147, v148\n"
    "v_lshlrev_b32 v147, 16, v125\n"
    "v_and_b32 v148, 0xffff0000, v125\n"
    "v_fma_f32 v149, v147, v145, -v146\n"
    "v_fma_f32 v150, v148, v145, -v146\n"
    "v_fma_f32 v147, v147, v151, -v13\n"
    "v_fma_f32 v148, v148, v151, -v13\n"
    "v_cvt_pk_bf16_f32 v131, v149, v150\n"
    "v_cvt_pk_bf16_f32 v135, v147, v148\n"
    "ds_write_b128 v140, v[128:131]\n"
    "ds_write_b128 v140, v[132:135] offset:6144\n"
    "s_add_u32 m0, s81, 4096\n"
    "s_nop 0\n"
    "buffer_load_dwordx4 v4, s[44:47], s78 offen lds\n"
    "buffer_load_dwordx4 v4, s[44:47], s78 offen offset:1024 lds\n"
    "s_add_u32 m0, s81, 6144\n"
    "s_nop 0\n"
    "buffer_load_dwordx4 v4, s[44:47], s79 offen lds\n"
    "buffer_load_dwordx4 v4, s[44:47], s79 offen offset:1024 lds\n"
    "s_waitcnt lgkmcnt(0)\n"
    "s_barrier\n"
    "ds_read_b64_tr_b16 v[16:17], v12 offset:0\n"
    "ds_read_b64_tr_b16 v[18:19], v12 offset:512\n"
    "ds_read_b64_tr_b16 v[20:21], v12 offset:1024\n"
    "ds_read_b64_tr_b16 v[22:23], v12 offset:1536\n"
    "ds_read_b64_tr_b16 v[24:25], v12 offset:2048\n"
    "ds_read_b64_tr_b16 v[26:27], v12 offset:2560\n"
    "ds_read_b64_tr_b16 v[28:29], v12 offset:3072\n"
    "ds_read_b64_tr_b16 v[30:31], v12 offset:3584\n"
    "ds_read_b64_tr_b16 v[32:33], v12 offset:4096\n"
    "ds_read_b64_tr_b16 v[34:35], v12 offset:4608\n"
    "ds_read_b64_tr_b16 v[36:37], v12 offset:5120\n"
    "ds_read_b64_tr_b16 v[38:39], v12 offset:5632\n"
    "s_min_i32 s34, s99, 0\n"
    "s_add_u32 s34, s34, s98\n"
    "s_add_u32 m0, s34, 131072\n"
    "s_nop 0\n"
    "buffer_load_dwordx4 v4, s[48:51], s34 offen lds\n"
    "s_cmp_gt_i32 s99, 0\n"
    "s_cbranch_scc0 .Lmx_nob2\n"
    "s_add_u32 s34, s98, s99\n"
    "s_add_u32 m0, s34, 131072\n"
    "s_nop 0\n"
    "buffer_load_dwordx4 v4, s[48:51], s34 offen lds\n"
    ".Lmx_nob2:\n"
    "s_waitcnt vmcnt(7)\n"
    "ds_read_u16 v176, v11 offset:0\n"
    "ds_read_b64_tr_b16 v[160:161], v5 offset:0\n"
    "ds_read_b64_tr_b16 v[162:163], v6 offset:1024\n"
    "ds_read_b64_tr_b16 v[164:165], v5 offset:2048\n"
    "ds_read_b64_tr_b16 v[166:167], v6 offset:3072\n"
    "s_waitcnt lgkmcnt(4)\n"
    "v_mfma_f32_32x32x16_bf16 v[88:103], v[176:179], v[180:183], 0\n"
    "s_waitcnt lgkmcnt(2)\n"
    "v_mfma_f32_32x32x16_bf16 v[104:119], v[160:163], v[16:19], v[88:103]\n"
    "s_waitcnt lgkmcnt(0)\n"
    "v_mfma_f32_32x32x16_bf16 v[104:119], v[164:167], v[20:23], v[104:119]\n"
    "s_cmp_lt_u32 s68, 4\n"
    "s_mov_b32 s86, 0xdddddddd\n"
    "s_cselect_b32 s86, 0x88888888, s86\n"
    "s_waitcnt lgkmcnt(0)\n"
    "v_mov_b32 v12, s86\n"
    "s_waitcnt vmcnt(5)\n"
    "s_barrier\n"
    "s_nop 7\n"
    "s_nop 3\n"
    "s_and_b32 s34, s68, 3\n"
    "v_lshrrev_b32 v238, 5, v1\n"
    "v_lshlrev_b32 v238, 3, v238\n"
    "s_lshl_b32 s34, s34, 4\n"
    "buffer_load_dwordx2 v[232:233], v238, s[60:63], s34 offen\n"
    "s_cmp_lg_u32 s68, s94\n"
    "s_cbranch_scc1 .Lmx_nb2s\n"
    "v_lshrrev_b32 v238, 3, v1\n"
    "v_lshlrev_b32 v238, 3, v238\n"
    "buffer_load_dwordx2 v[234:235], v238, s[60:63], 0 offen\n"
    ".Lmx_nb2s:\n"
    "s_lshl_b32 s34, s73, 11\n"
    "s_add_u32 s34, s34, 1049600\n"
    "buffer_load_dword v240, off, s[56:59], s34 sc1\n"
    "s_cmp_lt_u32 s68, 4\n"
    "s_cbranch_scc0 .Lmx_npw\n"
    "s_and_b32 s34, s68, 1\n"
    "s_lshl_b32 s34, s34, 6\n"
    "v_add_u32 v236, s34, v1\n"
    "v_min_u32 v236, 71, v236\n"
    "v_lshlrev_b32 v236, 7, v236\n"
    "s_lshr_b32 s34, %1, 3\n"
    "s_mul_i32 s34, s34, 9216\n"
    "s_cmp_lt_u32 s68, 2\n"
    "s_cbranch_scc0 .Lmx_pw2\n"
    "buffer_load_dword v237, v236, s[40:43], s34 offen\n"
    "s_branch .Lmx_npw\n"
    ".Lmx_pw2:\n"
    "buffer_load_dword v237, v236, s[44:47], s34 offen\n"
    ".Lmx_npw:\n"
    "s_cmp_eq_u32 s68, 4\n"
    "s_cbranch_scc0 .Lmx_npo\n"
    "buffer_load_dword v237, off, s[52:55], s72\n"
    ".Lmx_npo:\n"
    "s_cmp_lt_u32 s68, 4\n"
    "s_cbranch_scc1 .Lmx_L\n"
    "s_waitcnt vmcnt(5)\n"
    "ds_read_b64_tr_b16 v[168:169], v9 offset:0\n"
    "ds_read_b64_tr_b16 v[170:171], v9 offset:256\n"
    "ds_read_b64_tr_b16 v[172:173], v9 offset:1024\n"
    "ds_read_b64_tr_b16 v[174:175], v9 offset:1280\n"
    ".p2align 6\n"
    ".Lmx_p1:\n"
    "v_pk_fma_f32 v[136:137], v[104:105], v[104:105], s[82:83]\n"
    "v_pk_fma_f32 v[138:139], v[106:107], v[106:107], s[82:83]\n"
    "v_pk_fma_f32 v[140:141], v[108:109], v[108:109], s[82:83]\n"
    "v_pk_fma_f32 v[142:143], v[110:111], v[110:111], s[82:83]\n"
    "v_pk_fma_f32 v[144:145], v[112:113], v[112:113], s[82:83]\n"
    "v_pk_fma_f32 v[146:147], v[114:115], v[114:115], s[82:83]\n"
    "v_pk_fma_f32 v[148:149], v[116:117], v[116:117], s[82:83]\n"
    "v_pk_fma_f32 v[150:151], v[118:119], v[118:119], s[82:83]\n"
    "v_pk_mul_f32 v[136:137], v[104:105], v[136:137] neg_lo:[1,0] neg_hi:[1,0]\n"
    "v_pk_mul_f32 v[138:139], v[106:107], v[138:139] neg_lo:[1,0] neg_hi:[1,0]\n"
    "v_pk_mul_f32 v[140:141], v[108:109], v[140:141] neg_lo:[1,0] neg_hi:[1,0]\n"
    "v_pk_mul_f32 v[142:143], v[110:111], v[142:143] neg_lo:[1,0] neg_hi:[1,0]\n"
    "v_pk_mul_f32 v[144:145], v[112:113], v[144:145] neg_lo:[1,0] neg_hi:[1,0]\n"
    "v_pk_mul_f32 v[146:147], v[114:115], v[146:147] neg_lo:[1,0] neg_hi:[1,0]\n"
    "v_pk_mul_f32 v[148:149], v[116:117], v[148:149] neg_lo:[1,0] neg_hi:[1,0]\n"
    "v_pk_mul_f32 v[150:151], v[118:119], v[150:151] neg_lo:[1,0] neg_hi:[1,0]\n"
    "v_smfmac_f32_32x32x32_bf16 v[72:87], v[192:195], v[200:207], v12\n"
    "v_exp_f32 v136, v136\n"
    "s_add_u32 s74, s74, 1\n"
    "s_cmp_eq_u32 s74, 7\n"
    "v_exp_f32 v137, v137\n"
    "s_cselect_b32 s74, 0, s74\n"
    "s_add_u32 s86, s92, s74\n"
    "v_exp_f32 v138, v138\n"
    "s_lshl_b32 s76, s86, 7\n"
    "v_exp_f32 v139, v139\n"
    "v_smfmac_f32_32x32x32_bf16 v[72:87], v[196:199], v[208:215], v12\n"
    "v_exp_f32 v140, v140\n"
    "s_add_u32 s77, s76, 145408\n"
    "s_lshl_b32 s78, s86, 12\n"
    "v_exp_f32 v141, v141\n"
    "s_add_u32 s79, s78, 2048\n"
    "v_exp_f32 v142, v142\n"
    "s_lshl_b32 s85, s74, 7\n"
    "v_exp_f32 v143, v143\n"
    "v_exp_f32 v144, v144\n"
    "v_exp_f32 v145, v145\n"
    "v_exp_f32 v146, v146\n"
    "v_exp_f32 v147, v147\n"
    "v_exp_f32 v148, v148\n"
    "v_exp_f32 v149, v149\n"
    "v_exp_f32 v150, v150\n"
    "v_exp_f32 v151, v151\n"
    "v_pk_fma_f32 v[136:137], v[136:137], s[96:97], s[96:97]\n"
    "v_pk_fma_f32 v[138:139], v[138:139], s[96:97], s[96:97]\n"
    "v_pk_fma_f32 v[140:141], v[140:141], s[96:97], s[96:97]\n"
    "v_pk_fma_f32 v[142:143], v[142:143], s[96:97], s[96:97]\n"
    "v_pk_fma_f32 v[144:145], v[144:145], s[96:97], s[96:97]\n"
    "v_pk_fma_f32 v[146:147], v[146:147], s[96:97], s[96:97]\n"
    "v_pk_fma_f32 v[148:149], v[148:149], s[96:97], s[96:97]\n"
    "v_pk_fma_f32 v[150:151], v[150:151], s[96:97], s[96:97]\n"
    "v_mfma_f32_32x32x16_bf16 v[120:135], v[160:163], v[24:27], v[88:103]\n"
    "v_rcp_f32 v136, v136\n"
    "v_rcp_f32 v137, v137\n"
    "v_rcp_f32 v138, v138\n"
    "v_rcp_f32 v139, v139\n"
    "v_mfma_f32_32x32x16_bf16 v[120:135], v[164:167], v[28:31], v[120:135]\n"
    "v_rcp_f32 v140, v140\n"
    "s_setprio 3\n"
    "s_waitcnt vmcnt(0)\n"
    "ds_read_b64_tr_b16 v[192:193], v9 offset:2048\n"
    "ds_read_b64_tr_b16 v[194:195], v9 offset:2304\n"
    "ds_read_b64_tr_b16 v[196:197], v9 offset:3072\n"
    "ds_read_b64_tr_b16 v[198:199], v9 offset:3328\n"
    "s_setprio 0\n"
    "v_rcp_f32 v141, v141\n"
    "v_rcp_f32 v142, v142\n"
    "v_rcp_f32 v143, v143\n"
    "v_rcp_f32 v144, v144\n"
    "v_rcp_f32 v145, v145\n"
    "v_rcp_f32 v146, v146\n"
    "v_rcp_f32 v147, v147\n"
    "v_rcp_f32 v148, v148\n"
    "v_rcp_f32 v149, v149\n"
    "v_rcp_f32 v150, v150\n"
    "v_rcp_f32 v151, v151\n"
    "v_pk_mul_f32 v[136:137], v[104:105], v[136:137]\n"
    "v_pk_mul_f32 v[138:139], v[106:107], v[138:139]\n"
    "v_pk_mul_f32 v[140:141], v[108:109], v[140:141]\n"
    "v_pk_mul_f32 v[142:143], v[110:111], v[142:143]\n"
    "v_pk_mul_f32 v[144:145], v[112:113], v[144:145]\n"
    "v_pk_mul_f32 v[146:147], v[114:115], v[146:147]\n"
    "v_pk_mul_f32 v[148:149], v[116:117], v[148:149]\n"
    "v_pk_mul_f32 v[150:151], v[118:119], v[150:151]\n"
    "v_pk_fma_f32 v[200:201], v[120:121], v[120:121], s[82:83]\n"
    "v_pk_fma_f32 v[202:203], v[122:123], v[122:123], s[82:83]\n"
    "v_pk_fma_f32 v[204:205], v[124:125], v[124:125], s[82:83]\n"
    "v_pk_fma_f32 v[206:207], v[126:127], v[126:127], s[82:83]\n"
    "v_pk_fma_f32 v[208:209], v[128:129], v[128:129], s[82:83]\n"
    "v_pk_fma_f32 v[210:211], v[130:131], v[130:131], s[82:83]\n"
    "v_pk_fma_f32 v[212:213], v[132:133], v[132:133], s[82:83]\n"
    "v_pk_fma_f32 v[214:215], v[134:135], v[134:135], s[82:83]\n"
    "v_pk_mul_f32 v[200:201], v[120:121], v[200:201] neg_lo:[1,0] neg_hi:[1,0]\n"
    "v_pk_mul_f32 v[202:203], v[122:123], v[202:203] neg_lo:[1,0] neg_hi:[1,0]\n"
    "v_pk_mul_f32 v[204:205], v[124:125], v[204:205] neg_lo:[1,0] neg_hi:[1,0]\n"
    "v_pk_mul_f32 v[206:207], v[126:127], v[206:207] neg_lo:[1,0] neg_hi:[1,0]\n"
    "v_pk_mul_f32 v[208:209], v[128:129], v[208:209] neg_lo:[1,0] neg_hi:[1,0]\n"
    "v_pk_mul_f32 v[210:211], v[130:131], v[210:211] neg_lo:[1,0] neg_hi:[1,0]\n"
    "v_pk_mul_f32 v[212:213], v[132:133], v[212:213] neg_lo:[1,0] neg_hi:[1,0]\n"
    "v_pk_mul_f32 v[214:215], v[134:135], v[214:215] neg_lo:[1,0] neg_hi:[1,0]\n"
    "s_waitcnt lgkmcnt(6)\n"
    "v_smfmac_f32_32x32x32_bf16 v[40:55], v[168:171], v[136:143], v12\n"
    "v_exp_f32 v200, v200\n"
    "v_exp_f32 v201, v201\n"
    "v_exp_f32 v202, v202\n"
    "v_exp_f32 v203, v203\n"
    "s_waitcnt lgkmcnt(4)\n"
    "v_smfmac_f32_32x32x32_bf16 v[40:55], v[172:175], v[144:151], v12\n"
    "v_exp_f32 v204, v204\n"
    "v_exp_f32 v205, v205\n"
    "s_add_u32 m0, s81, 4096\n"
    "v_exp_f32 v206, v206\n"
    "buffer_load_dwordx4 v4, s[44:47], s78 offen lds\n"
    "v_exp_f32 v207, v207\n"
    "v_exp_f32 v208, v208\n"
    "buffer_load_dwordx4 v4, s[44:47], s78 offen offset:1024 lds\n"
    "v_exp_f32 v209, v209\n"
    "v_exp_f32 v210, v210\n"
    "v_exp_f32 v211, v211\n"
    "v_exp_f32 v212, v212\n"
    "v_exp_f32 v213, v213\n"
    "v_exp_f32 v214, v214\n"
    "v_exp_f32 v215, v215\n"
    "v_pk_fma_f32 v[200:201], v[200:201], s[96:97], s[96:97]\n"
    "v_pk_fma_f32 v[202:203], v[202:203], s[96:97], s[96:97]\n"
    "v_pk_fma_f32 v[204:205], v[204:205], s[96:97], s[96:97]\n"
    "v_pk_fma_f32 v[206:207], v[206:207], s[96:97], s[96:97]\n"
    "v_pk_fma_f32 v[208:209], v[208:209], s[96:97], s[96:97]\n"
    "v_pk_fma_f32 v[210:211], v[210:211], s[96:97], s[96:97]\n"
    "v_pk_fma_f32 v[212:213], v[212:213], s[96:97], s[96:97]\n"
    "v_pk_fma_f32 v[214:215], v[214:215], s[96:97], s[96:97]\n"
    "v_mfma_f32_32x32x16_bf16 v[104:119], v[160:163], v[32:35], v[88:103]\n"
    "v_rcp_f32 v200, v200\n"
    "v_rcp_f32 v201, v201\n"
    "v_rcp_f32 v202, v202\n"
    "v_rcp_f32 v203, v203\n"
    "v_mfma_f32_32x32x16_bf16 v[104:119], v[164:167], v[36:39], v[104:119]\n"
    "v_rcp_f32 v204, v204\n"
    "s_setprio 3\n"
    "ds_read_u16 v176, v11 offset:64\n"
    "ds_read_b64_tr_b16 v[184:185], v7 offset:0\n"
    "ds_read_b64_tr_b16 v[186:187], v8 offset:1024\n"
    "ds_read_b64_tr_b16 v[188:189], v7 offset:2048\n"
    "ds_read_b64_tr_b16 v[190:191], v8 offset:3072\n"
    "s_setprio 0\n"
    "v_rcp_f32 v205, v205\n"
    "v_rcp_f32 v206, v206\n"
    "v_rcp_f32 v207, v207\n"
    "v_rcp_f32 v208, v208\n"
    "v_rcp_f32 v209, v209\n"
    "v_rcp_f32 v210, v210\n"
    "v_rcp_f32 v211, v211\n"
    "v_rcp_f32 v212, v212\n"
    "v_rcp_f32 v213, v213\n"
    "v_rcp_f32 v214, v214\n"
    "v_rcp_f32 v215, v215\n"
    "v_pk_mul_f32 v[200:201], v[120:121], v[200:201]\n"
    "v_pk_mul_f32 v[202:203], v[122:123], v[202:203]\n"
    "v_pk_mul_f32 v[204:205], v[124:125], v[204:205]\n"
    "v_pk_mul_f32 v[206:207], v[126:127], v[206:207]\n"
    "v_pk_mul_f32 v[208:209], v[128:129], v[208:209]\n"
    "v_pk_mul_f32 v[210:211], v[130:131], v[210:211]\n"
    "v_pk_mul_f32 v[212:213], v[132:133], v[212:213]\n"
    "v_pk_mul_f32 v[214:215], v[134:135], v[214:215]\n"
    "v_pk_fma_f32 v[136:137], v[104:105], v[104:105], s[82:83]\n"
    "v_pk_fma_f32 v[138:139], v[106:107], v[106:107], s[82:83]\n"
    "v_pk_fma_f32 v[140:141], v[108:109], v[108:109], s[82:83]\n"
    "v_pk_fma_f32 v[142:143], v[110:111], v[110:111], s[82:83]\n"
    "v_pk_fma_f32 v[144:145], v[112:113], v[112:113], s[82:83]\n"
    "v_pk_fma_f32 v[146:147], v[114:115], v[114:115], s[82:83]\n"
    "v_pk_fma_f32 v[148:149], v[116:117], v[116:117], s[82:83]\n"
    "v_pk_fma_f32 v[150:151], v[118:119], v[118:119], s[82:83]\n"
    "v_pk_mul_f32 v[136:137], v[104:105], v[136:137] neg_lo:[1,0] neg_hi:[1,0]\n"
    "v_pk_mul_f32 v[138:139], v[106:107], v[138:139] neg_lo:[1,0] neg_hi:[1,0]\n"
    "v_pk_mul_f32 v[140:141], v[108:109], v[140:141] neg_lo:[1,0] neg_hi:[1,0]\n"
    "v_pk_mul_f32 v[142:143], v[110:111], v[142:143] neg_lo:[1,0] neg_hi:[1,0]\n"
    "v_pk_mul_f32 v[144:145], v[112:113], v[144:145] neg_lo:[1,0] neg_hi:[1,0]\n"
    "v_pk_mul_f32 v[146:147], v[114:115], v[146:147] neg_lo:[1,0] neg_hi:[1,0]\n"
    "v_pk_mul_f32 v[148:149], v[116:117], v[148:149] neg_lo:[1,0] neg_hi:[1,0]\n"
    "v_pk_mul_f32 v[150:151], v[118:119], v[150:151] neg_lo:[1,0] neg_hi:[1,0]\n"
    "v_smfmac_f32_32x32x32_bf16 v[56:71], v[168:171], v[200:207], v12\n"
    "v_exp_f32 v136, v136\n"
    "v_exp_f32 v137, v137\n"
    "v_exp_f32 v138, v138\n"
    "v_exp_f32 v139, v139\n"
    "v_smfmac_f32_32x32x32_bf16 v[56:71], v[172:175], v[208:215], v12\n"
    "v_exp_f32 v140, v140\n"
    "v_exp_f32 v141, v141\n"
    "v_exp_f32 v142, v142\n"
    "v_exp_f32 v143, v143\n"
    "v_exp_f32 v144, v144\n"
    "v_exp_f32 v145, v145\n"
    "v_exp_f32 v146, v146\n"
    "s_mov_b32 m0, s81\n"
    "v_exp_f32 v147, v147\n"
    "s_waitcnt lgkmcnt(0)\n"
    "buffer_load_dwordx4 v2, s[40:43], s76 offen lds\n"
    "v_exp_f32 v148, v148\n"
    "buffer_load_dwordx4 v3, s[40:43], s76 offen offset:1024 lds\n"
    "v_exp_f32 v149, v149\n"
    "v_exp_f32 v150, v150\n"
    "buffer_load_dwordx4 v2, s[40:43], s77 offen offset:2048 lds\n"
    "v_exp_f32 v151, v151\n"
    "buffer_load_dwordx4 v3, s[40:43], s77 offen offset:3072 lds\n"
    "v_pk_fma_f32 v[136:137], v[136:137], s[96:97], s[96:97]\n"
    "v_pk_fma_f32 v[138:139], v[138:139], s[96:97], s[96:97]\n"
    "v_pk_fma_f32 v[140:141], v[140:141], s[96:97], s[96:97]\n"
    "v_pk_fma_f32 v[142:143], v[142:143], s[96:97], s[96:97]\n"
    "v_pk_fma_f32 v[144:145], v[144:145], s[96:97], s[96:97]\n"
    "v_pk_fma_f32 v[146:147], v[146:147], s[96:97], s[96:97]\n"
    "v_pk_fma_f32 v[148:149], v[148:149], s[96:97], s[96:97]\n"
    "v_pk_fma_f32 v[150:151], v[150:151], s[96:97], s[96:97]\n"
    "v_mfma_f32_32x32x16_bf16 v[88:103], v[176:179], v[180:183], 0\n"
    "v_rcp_f32 v136, v136\n"
    "v_rcp_f32 v137, v137\n"
    "v_rcp_f32 v138, v138\n"
    "v_rcp_f32 v139, v139\n"
    "v_mfma_f32_32x32x16_bf16 v[120:135], v[184:187], v[16:19], v[88:103]\n"
    "v_rcp_f32 v140, v140\n"
    "v_rcp_f32 v141, v141\n"
    "v_rcp_f32 v142, v142\n"
    "v_rcp_f32 v143, v143\n"
    "v_mfma_f32_32x32x16_bf16 v[120:135], v[188:191], v[20:23], v[120:135]\n"
    "v_rcp_f32 v144, v144\n"
    "v_rcp_f32 v145, v145\n"
    "v_rcp_f32 v146, v146\n"
    "v_rcp_f32 v147, v147\n"
    "v_rcp_f32 v148, v148\n"
    "v_rcp_f32 v149, v149\n"
    "v_rcp_f32 v150, v150\n"
    "v_rcp_f32 v151, v151\n"
    "v_pk_mul_f32 v[136:137], v[104:105], v[136:137]\n"
    "v_pk_mul_f32 v[138:139], v[106:107], v[138:139]\n"
    "v_pk_mul_f32 v[140:141], v[108:109], v[140:141]\n"
    "v_pk_mul_f32 v[142:143], v[110:111], v[142:143]\n"
    "v_pk_mul_f32 v[144:145], v[112:113], v[144:145]\n"
    "v_pk_mul_f32 v[146:147], v[114:115], v[146:147]\n"
    "v_pk_mul_f32 v[148:149], v[116:117], v[148:149]\n"
    "v_pk_mul_f32 v[150:151], v[118:119], v[150:151]\n"
    "v_pk_fma_f32 v[200:201], v[120:121], v[120:121], s[82:83]\n"
    "v_pk_fma_f32 v[202:203], v[122:123], v[122:123], s[82:83]\n"
    "v_pk_fma_f32 v[204:205], v[124:125], v[124:125], s[82:83]\n"
    "v_pk_fma_f32 v[206:207], v[126:127], v[126:127], s[82:83]\n"
    "v_pk_fma_f32 v[208:209], v[128:129], v[128:129], s[82:83]\n"
    "v_pk_fma_f32 v[210:211], v[130:131], v[130:131], s[82:83]\n"
    "v_pk_fma_f32 v[212:213], v[132:133], v[132:133], s[82:83]\n"
    "v_pk_fma_f32 v[214:215], v[134:135], v[134:135], s[82:83]\n"
    "v_pk_mul_f32 v[200:201], v[120:121], v[200:201] neg_lo:[1,0] neg_hi:[1,0]\n"
    "v_pk_mul_f32 v[202:203], v[122:123], v[202:203] neg_lo:[1,0] neg_hi:[1,0]\n"
    "v_pk_mul_f32 v[204:205], v[124:125], v[204:205] neg_lo:[1,0] neg_hi:[1,0]\n"
    "v_pk_mul_f32 v[206:207], v[126:127], v[206:207] neg_lo:[1,0] neg_hi:[1,0]\n"
    "v_pk_mul_f32 v[208:209], v[128:129], v[208:209] neg_lo:[1,0] neg_hi:[1,0]\n"
    "v_pk_mul_f32 v[210:211], v[130:131], v[210:211] neg_lo:[1,0] neg_hi:[1,0]\n"
    "v_pk_mul_f32 v[212:213], v[132:133], v[212:213] neg_lo:[1,0] neg_hi:[1,0]\n"
    "v_pk_mul_f32 v[214:215], v[134:135], v[214:215] neg_lo:[1,0] neg_hi:[1,0]\n"
    "v_smfmac_f32_32x32x32_bf16 v[72:87], v[168:171], v[136:143], v12\n"
    "v_exp_f32 v200, v200\n"
    "v_exp_f32 v201, v201\n"
    "v_exp_f32 v202, v202\n"
    "v_exp_f32 v203, v203\n"
    "v_smfmac_f32_32x32x32_bf16 v[72:87], v[172:175], v[144:151], v12\n"
    "v_exp_f32 v204, v204\n"
    "v_exp_f32 v205, v205\n"
    "v_add_u32 v11, s85, v10\n"
    "v_exp_f32 v206, v206\n"
    "v_exp_f32 v207, v207\n"
    "v_exp_f32 v208, v208\n"
    "v_exp_f32 v209, v209\n"
    "v_exp_f32 v210, v210\n"
    "v_exp_f32 v211, v211\n"
    "v_exp_f32 v212, v212\n"
    "v_exp_f32 v213, v213\n"
    "v_exp_f32 v214, v214\n"
    "v_exp_f32 v215, v215\n"
    "v_pk_fma_f32 v[200:201], v[200:201], s[96:97], s[96:97]\n"
    "v_pk_fma_f32 v[202:203], v[202:203], s[96:97], s[96:97]\n"
    "v_pk_fma_f32 v[204:205], v[204:205], s[96:97], s[96:97]\n"
    "v_pk_fma_f32 v[206:207], v[206:207], s[96:97], s[96:97]\n"
    "v_pk_fma_f32 v[208:209], v[208:209], s[96:97], s[96:97]\n"
    "v_pk_fma_f32 v[210:211], v[210:211], s[96:97], s[96:97]\n"
    "v_pk_fma_f32 v[212:213], v[212:213], s[96:97], s[96:97]\n"
    "v_pk_fma_f32 v[214:215], v[214:215], s[96:97], s[96:97]\n"
    "v_mfma_f32_32x32x16_bf16 v[104:119], v[184:187], v[24:27], v[88:103]\n"
    "v_rcp_f32 v200, v200\n"
    "v_rcp_f32 v201, v201\n"
    "v_rcp_f32 v202, v202\n"
    "v_rcp_f32 v203, v203\n"
    "v_mfma_f32_32x32x16_bf16 v[104:119], v[188:191], v[28:31], v[104:119]\n"
    "v_rcp_f32 v204, v204\n"
    "s_setprio 3\n"
    "s_waitcnt vmcnt(4)\n"
    "ds_read_b64_tr_b16 v[168:169], v9 offset:0\n"
    "ds_read_b64_tr_b16 v[170:171], v9 offset:256\n"
    "ds_read_b64_tr_b16 v[172:173], v9 offset:1024\n"
    "ds_read_b64_tr_b16 v[174:175], v9 offset:1280\n"
    "s_setprio 0\n"
    "v_rcp_f32 v205, v205\n"
    "v_rcp_f32 v206, v206\n"
    "v_rcp_f32 v207, v207\n"
    "v_rcp_f32 v208, v208\n"
    "v_rcp_f32 v209, v209\n"
    "v_rcp_f32 v210, v210\n"
    "v_rcp_f32 v211, v211\n"
    "v_rcp_f32 v212, v212\n"
    "v_rcp_f32 v213, v213\n"
    "v_rcp_f32 v214, v214\n"
    "v_rcp_f32 v215, v215\n"
    "v_pk_mul_f32 v[200:201], v[120:121], v[200:201]\n"
    "v_pk_mul_f32 v[202:203], v[122:123], v[202:203]\n"
    "v_pk_mul_f32 v[204:205], v[124:125], v[204:205]\n"
    "v_pk_mul_f32 v[206:207], v[126:127], v[206:207]\n"
    "v_pk_mul_f32 v[208:209], v[128:129], v[208:209]\n"
    "v_pk_mul_f32 v[210:211], v[130:131], v[210:211]\n"
    "v_pk_mul_f32 v[212:213], v[132:133], v[212:213]\n"
    "v_pk_mul_f32 v[214:215], v[134:135], v[214:215]\n"
    "v_pk_fma_f32 v[136:137], v[104:105], v[104:105], s[82:83]\n"
    "v_pk_fma_f32 v[138:139], v[106:107], v[106:107], s[82:83]\n"
    "v_pk_fma_f32 v[140:141], v[108:109], v[108:109], s[82:83]\n"
    "v_pk_fma_f32 v[142:143], v[110:111], v[110:111], s[82:83]\n"
    "v_pk_fma_f32 v[144:145], v[112:113], v[112:113], s[82:83]\n"
    "v_pk_fma_f32 v[146:147], v[114:115], v[114:115], s[82:83]\n"
    "v_pk_fma_f32 v[148:149], v[116:117], v[116:117], s[82:83]\n"
    "v_pk_fma_f32 v[150:151], v[118:119], v[118:119], s[82:83]\n"
    "v_pk_mul_f32 v[136:137], v[104:105], v[136:137] neg_lo:[1,0] neg_hi:[1,0]\n"
    "v_pk_mul_f32 v[138:139], v[106:107], v[138:139] neg_lo:[1,0] neg_hi:[1,0]\n"
    "v_pk_mul_f32 v[140:141], v[108:109], v[140:141] neg_lo:[1,0] neg_hi:[1,0]\n"
    "v_pk_mul_f32 v[142:143], v[110:111], v[142:143] neg_lo:[1,0] neg_hi:[1,0]\n"
    "v_pk_mul_f32 v[144:145], v[112:113], v[144:145] neg_lo:[1,0] neg_hi:[1,0]\n"
    "v_pk_mul_f32 v[146:147], v[114:115], v[146:147] neg_lo:[1,0] neg_hi:[1,0]\n"
    "v_pk_mul_f32 v[148:149], v[116:117], v[148:149] neg_lo:[1,0] neg_hi:[1,0]\n"
    "v_pk_mul_f32 v[150:151], v[118:119], v[150:151] neg_lo:[1,0] neg_hi:[1,0]\n"
    "v_smfmac_f32_32x32x32_bf16 v[40:55], v[192:195], v[200:207], v12\n"
    "v_exp_f32 v136, v136\n"
    "v_exp_f32 v137, v137\n"
    "v_exp_f32 v138, v138\n"
    "v_exp_f32 v139, v139\n"
    "v_smfmac_f32_32x32x32_bf16 v[40:55], v[196:199], v[208:215], v12\n"
    "v_exp_f32 v140, v140\n"
    "v_exp_f32 v141, v141\n"
    "s_add_u32 m0, s81, 6144\n"
    "v_exp_f32 v142, v142\n"
    "buffer_load_dwordx4 v4, s[44:47], s79 offen lds\n"
    "v_exp_f32 v143, v143\n"
    "v_exp_f32 v144, v144\n"
    "buffer_load_dwordx4 v4, s[44:47], s79 offen offset:1024 lds\n"
    "v_exp_f32 v145, v145\n"
    "v_exp_f32 v146, v146\n"
    "v_exp_f32 v147, v147\n"
    "v_exp_f32 v148, v148\n"
    "v_exp_f32 v149, v149\n"
    "v_exp_f32 v150, v150\n"
    "v_exp_f32 v151, v151\n"
    "v_pk_fma_f32 v[136:137], v[136:137], s[96:97], s[96:97]\n"
    "v_pk_fma_f32 v[138:139], v[138:139], s[96:97], s[96:97]\n"
    "v_pk_fma_f32 v[140:141], v[140:141], s[96:97], s[96:97]\n"
    "v_pk_fma_f32 v[142:143], v[142:143], s[96:97], s[96:97]\n"
    "v_pk_fma_f32 v[144:145], v[144:145], s[96:97], s[96:97]\n"
    "v_pk_fma_f32 v[146:147], v[146:147], s[96:97], s[96:97]\n"
    "v_pk_fma_f32 v[148:149], v[148:149], s[96:97], s[96:97]\n"
    "v_pk_fma_f32 v[150:151], v[150:151], s[96:97], s[96:97]\n"
    "v_mfma_f32_32x32x16_bf16 v[120:135], v[184:187], v[32:35], v[88:103]\n"
    "v_rcp_f32 v136, v136\n"
    "v_rcp_f32 v137, v137\n"
    "v_rcp_f32 v138, v138\n"
    "v_rcp_f32 v139, v139\n"
    "v_mfma_f32_32x32x16_bf16 v[120:135], v[188:191], v[36:39], v[120:135]\n"
    "v_rcp_f32 v140, v140\n"
    "s_setprio 3\n"
    "s_waitcnt vmcnt(2)\n"
    "ds_read_u16 v176, v11 offset:0\n"
    "ds_read_b64_tr_b16 v[160:161], v5 offset:0\n"
    "ds_read_b64_tr_b16 v[162:163], v6 offset:1024\n"
    "ds_read_b64_tr_b16 v[164:165], v5 offset:2048\n"
    "ds_read_b64_tr_b16 v[166:167], v6 offset:3072\n"
    "s_setprio 0\n"
    "v_rcp_f32 v141, v141\n"
    "v_rcp_f32 v142, v142\n"
    "v_rcp_f32 v143, v143\n"
    "v_rcp_f32 v144, v144\n"
    "v_rcp_f32 v145, v145\n"
    "v_rcp_f32 v146, v146\n"
    "v_rcp_f32 v147, v147\n"
    "v_rcp_f32 v148, v148\n"
    "v_rcp_f32 v149, v149\n"
    "v_rcp_f32 v150, v150\n"
    "v_rcp_f32 v151, v151\n"
    "v_pk_mul_f32 v[136:137], v[104:105], v[136:137]\n"
    "v_pk_mul_f32 v[138:139], v[106:107], v[138:139]\n"
    "v_pk_mul_f32 v[140:141], v[108:109], v[140:141]\n"
    "v_pk_mul_f32 v[142:143], v[110:111], v[142:143]\n"
    "v_pk_mul_f32 v[144:145], v[112:113], v[144:145]\n"
    "v_pk_mul_f32 v[146:147], v[114:115], v[146:147]\n"
    "v_pk_mul_f32 v[148:149], v[116:117], v[148:149]\n"
    "v_pk_mul_f32 v[150:151], v[118:119], v[150:151]\n"
    "v_pk_fma_f32 v[200:201], v[120:121], v[120:121], s[82:83]\n"
    "v_pk_fma_f32 v[202:203], v[122:123], v[122:123], s[82:83]\n"
    "v_pk_fma_f32 v[204:205], v[124:125], v[124:125], s[82:83]\n"
    "v_pk_fma_f32 v[206:207], v[126:127], v[126:127], s[82:83]\n"
    "v_pk_fma_f32 v[208:209], v[128:129], v[128:129], s[82:83]\n"
    "v_pk_fma_f32 v[210:211], v[130:131], v[130:131], s[82:83]\n"
    "v_pk_fma_f32 v[212:213], v[132:133], v[132:133], s[82:83]\n"
    "v_pk_fma_f32 v[214:215], v[134:135], v[134:135], s[82:83]\n"
    "v_pk_mul_f32 v[200:201], v[120:121], v[200:201] neg_lo:[1,0] neg_hi:[1,0]\n"
    "v_pk_mul_f32 v[202:203], v[122:123], v[202:203] neg_lo:[1,0] neg_hi:[1,0]\n"
    "v_pk_mul_f32 v[204:205], v[124:125], v[204:205] neg_lo:[1,0] neg_hi:[1,0]\n"
    "v_pk_mul_f32 v[206:207], v[126:127], v[206:207] neg_lo:[1,0] neg_hi:[1,0]\n"
    "v_pk_mul_f32 v[208:209], v[128:129], v[208:209] neg_lo:[1,0] neg_hi:[1,0]\n"
    "v_pk_mul_f32 v[210:211], v[130:131], v[210:211] neg_lo:[1,0] neg_hi:[1,0]\n"
    "v_pk_mul_f32 v[212:213], v[132:133], v[212:213] neg_lo:[1,0] neg_hi:[1,0]\n"
    "v_pk_mul_f32 v[214:215], v[134:135], v[214:215] neg_lo:[1,0] neg_hi:[1,0]\n"
    "v_smfmac_f32_32x32x32_bf16 v[56:71], v[192:195], v[136:143], v12\n"
    "v_exp_f32 v200, v200\n"
    "v_exp_f32 v201, v201\n"
    "v_exp_f32 v202, v202\n"
    "v_exp_f32 v203, v203\n"
    "v_smfmac_f32_32x32x32_bf16 v[56:71], v[196:199], v[144:151], v12\n"
    "v_exp_f32 v204, v204\n"
    "v_exp_f32 v205, v205\n"
    "v_exp_f32 v206, v206\n"
    "v_exp_f32 v207, v207\n"
    "v_exp_f32 v208, v208\n"
    "v_exp_f32 v209, v209\n"
    "v_exp_f32 v210, v210\n"
    "v_exp_f32 v211, v211\n"
    "v_exp_f32 v212, v212\n"
    "v_exp_f32 v213, v213\n"
    "v_exp_f32 v214, v214\n"
    "v_exp_f32 v215, v215\n"
    "v_pk_fma_f32 v[200:201], v[200:201], s[96:97], s[96:97]\n"
    "v_pk_fma_f32 v[202:203], v[202:203], s[96:97], s[96:97]\n"
    "v_pk_fma_f32 v[204:205], v[204:205], s[96:97], s[96:97]\n"
    "v_pk_fma_f32 v[206:207], v[206:207], s[96:97], s[96:97]\n"
    "v_pk_fma_f32 v[208:209], v[208:209], s[96:97], s[96:97]\n"
    "v_pk_fma_f32 v[210:211], v[210:211], s[96:97], s[96:97]\n"
    "v_pk_fma_f32 v[212:213], v[212:213], s[96:97], s[96:97]\n"
    "v_pk_fma_f32 v[214:215], v[214:215], s[96:97], s[96:97]\n"
    "s_waitcnt lgkmcnt(4)\n"
    "v_mfma_f32_32x32x16_bf16 v[88:103], v[176:179], v[180:183], 0\n"
    "v_rcp_f32 v200, v200\n"
    "v_rcp_f32 v201, v201\n"
    "v_rcp_f32 v202, v202\n"
    "v_rcp_f32 v203, v203\n"
    "s_waitcnt lgkmcnt(2)\n"
    "v_mfma_f32_32x32x16_bf16 v[104:119], v[160:163], v[16:19], v[88:103]\n"
    "v_rcp_f32 v204, v204\n"
    "v_rcp_f32 v205, v205\n"
    "v_rcp_f32 v206, v206\n"
    "v_rcp_f32 v207, v207\n"
    "s_waitcnt lgkmcnt(0)\n"
    "v_mfma_f32_32x32x16_bf16 v[104:119], v[164:167], v[20:23], v[104:119]\n"
    "v_rcp_f32 v208, v208\n"
    "v_rcp_f32 v209, v209\n"
    "v_rcp_f32 v210, v210\n"
    "v_rcp_f32 v211, v211\n"
    "v_rcp_f32 v212, v212\n"
    "v_rcp_f32 v213, v213\n"
    "v_rcp_f32 v214, v214\n"
    "v_rcp_f32 v215, v215\n"
    "v_pk_mul_f32 v[200:201], v[120:121], v[200:201]\n"
    "v_pk_mul_f32 v[202:203], v[122:123], v[202:203]\n"
    "v_pk_mul_f32 v[204:205], v[124:125], v[204:205]\n"
    "v_pk_mul_f32 v[206:207], v[126:127], v[206:207]\n"
    "v_pk_mul_f32 v[208:209], v[128:129], v[208:209]\n"
    "v_pk_mul_f32 v[210:211], v[130:131], v[210:211]\n"
    "v_pk_mul_f32 v[212:213], v[132:133], v[212:213]\n"
    "v_pk_mul_f32 v[214:215], v[134:135], v[214:215]\n"
    "s_sub_u32 s75, s75, 1\n"
    "s_cmp_lg_u32 s75, 0\n"
    "s_cbranch_scc1 .Lmx_p1\n"
    "v_smfmac_f32_32x32x32_bf16 v[72:87], v[192:195], v[200:207], v12\n"
    "v_smfmac_f32_32x32x32_bf16 v[72:87], v[196:199], v[208:215], v12\n"
    "v_mov_b32 v200, 0\n"
    "v_mov_b32 v201, 0\n"
    "v_mov_b32 v202, 0\n"
    "v_mov_b32 v203, 0\n"
    "v_mov_b32 v204, 0\n"
    "v_mov_b32 v205, 0\n"
    "v_mov_b32 v206, 0\n"
    "v_mov_b32 v207, 0\n"
    "v_mov_b32 v208, 0\n"
    "v_mov_b32 v209, 0\n"
    "v_mov_b32 v210, 0\n"
    "v_mov_b32 v211, 0\n"
    "v_mov_b32 v212, 0\n"
    "v_mov_b32 v213, 0\n"
    "v_mov_b32 v214, 0\n"
    "v_mov_b32 v215, 0\n"
    "v_readfirstlane_b32 s80, v240\n"
    "s_add_u32 s80, s80, 1\n"
    "s_sub_u32 s86, s68, s69\n"
    "s_and_b32 s86, s86, 3\n"
    "s_mov_b32 s36, 1\n"
    "s_mov_b32 s37, 0\n"
    "s_mov_b32 s39, 0\n"
    "s_mov_b32 s38, 0\n"
    "s_cmp_eq_u32 s86, 3\n"
    "s_cbranch_scc1 .Lmx_tdv\n"
    "s_mov_b32 s36, 0\n"
    "s_cmp_eq_u32 s86, 2\n"
    "s_cbranch_scc1 .Lmx_fexp\n"
    "s_add_u32 s86, s86, 3\n"
    "s_lshl_b32 s86, s86, 12\n"
    "s_add_u32 s86, s86, 140288\n"
    "v_lshl_add_u32 v15, v1, 4, s86\n"
    "ds_write_b128 v15, v[72:75] offset:0\n"
    "ds_write_b128 v15, v[76:79] offset:1024\n"
    "ds_write_b128 v15, v[80:83] offset:2048\n"
    "ds_write_b128 v15, v[84:87] offset:3072\n"
    "v_mov_b32 v13, 162820\n"
    "v_mov_b32 v14, 1\n"
    "s_waitcnt lgkmcnt(0)\n"
    "s_mov_b64 exec, 1\n"
    "ds_add_u32 v13, v14\n"
    "s_mov_b64 exec, -1\n"
    "s_branch .Lmx_tdv\n"
    ".Lmx_fexp:\n"
    "s_lshl_b32 s88, s73, 14\n"
    "s_lshl_b32 s86, s69, 12\n"
    "s_add_u32 s88, s88, s86\n"
    "s_add_u32 s88, s88, 2097152\n"
    "buffer_store_dwordx4 v[72:75], v4, s[56:59], s88 offen offset:0 sc1\n"
    "buffer_store_dwordx4 v[76:79], v4, s[56:59], s88 offen offset:1024 sc1\n"
    "buffer_store_dwordx4 v[80:83], v4, s[56:59], s88 offen offset:2048 sc1\n"
    "buffer_store_dwordx4 v[84:87], v4, s[56:59], s88 offen offset:3072 sc1\n"
    "s_mov_b32 s93, 1\n"
    "s_mov_b32 s37, 128\n"
    ".Lmx_tdv:\n"
    ".p2align 6\n"
    ".Lmx_p2:\n"
    "v_pk_fma_f32 v[136:137], v[104:105], v[104:105], s[82:83]\n"
    "v_pk_fma_f32 v[138:139], v[106:107], v[106:107], s[82:83]\n"
    "v_pk_fma_f32 v[140:141], v[108:109], v[108:109], s[82:83]\n"
    "v_pk_fma_f32 v[142:143], v[110:111], v[110:111], s[82:83]\n"
    "v_pk_fma_f32 v[144:145], v[112:113], v[112:113], s[82:83]\n"
    "v_pk_fma_f32 v[146:147], v[114:115], v[114:115], s[82:83]\n"
    "v_pk_fma_f32 v[148:149], v[116:117], v[116:117], s[82:83]\n"
    "v_pk_fma_f32 v[150:151], v[118:119], v[118:119], s[82:83]\n"
    "v_pk_mul_f32 v[136:137], v[104:105], v[136:137] neg_lo:[1,0] neg_hi:[1,0]\n"
    "v_pk_mul_f32 v[138:139], v[106:107], v[138:139] neg_lo:[1,0] neg_hi:[1,0]\n"
    "v_pk_mul_f32 v[140:141], v[108:109], v[140:141] neg_lo:[1,0] neg_hi:[1,0]\n"
    "v_pk_mul_f32 v[142:143], v[110:111], v[142:143] neg_lo:[1,0] neg_hi:[1,0]\n"
    "v_pk_mul_f32 v[144:145], v[112:113], v[144:145] neg_lo:[1,0] neg_hi:[1,0]\n"
    "v_pk_mul_f32 v[146:147], v[114:115], v[146:147] neg_lo:[1,0] neg_hi:[1,0]\n"
    "v_pk_mul_f32 v[148:149], v[116:117], v[148:149] neg_lo:[1,0] neg_hi:[1,0]\n"
    "v_pk_mul_f32 v[150:151], v[118:119], v[150:151] neg_lo:[1,0] neg_hi:[1,0]\n"
    "v_smfmac_f32_32x32x32_bf16 v[56:71], v[192:195], v[200:207], v12\n"
    "v_exp_f32 v136, v136\n"
    "s_add_u32 s74, s74, 1\n"
    "s_cmp_eq_u32 s74, 7\n"
    "v_exp_f32 v137, v137\n"
    "s_cselect_b32 s74, 0, s74\n"
    "s_add_u32 s86, s92, s74\n"
    "v_exp_f32 v138, v138\n"
    "s_lshl_b32 s76, s86, 7\n"
    "v_exp_f32 v139, v139\n"
    "v_smfmac_f32_32x32x32_bf16 v[56:71], v[196:199], v[208:215], v12\n"
    "v_exp_f32 v140, v140\n"
    "s_add_u32 s77, s76, 145408\n"
    "s_lshl_b32 s78, s86, 12\n"
    "v_exp_f32 v141, v141\n"
    "s_add_u32 s79, s78, 2048\n"
    "v_exp_f32 v142, v142\n"
    "s_lshl_b32 s85, s74, 7\n"
    "v_exp_f32 v143, v143\n"
    "v_exp_f32 v144, v144\n"
    "v_exp_f32 v145, v145\n"
    "v_exp_f32 v146, v146\n"
    "v_exp_f32 v147, v147\n"
    "v_exp_f32 v148, v148\n"
    "v_exp_f32 v149, v149\n"
    "v_exp_f32 v150, v150\n"
    "v_exp_f32 v151, v151\n"
    "v_pk_fma_f32 v[136:137], v[136:137], s[96:97], s[96:97]\n"
    "v_pk_fma_f32 v[138:139], v[138:139], s[96:97], s[96:97]\n"
    "v_pk_fma_f32 v[140:141], v[140:141], s[96:97], s[96:97]\n"
    "v_pk_fma_f32 v[142:143], v[142:143], s[96:97], s[96:97]\n"
    "v_pk_fma_f32 v[144:145], v[144:145], s[96:97], s[96:97]\n"
    "v_pk_fma_f32 v[146:147], v[146:147], s[96:97], s[96:97]\n"
    "v_pk_fma_f32 v[148:149], v[148:149], s[96:97], s[96:97]\n"
    "v_pk_fma_f32 v[150:151], v[150:151], s[96:97], s[96:97]\n"
    "v_mfma_f32_32x32x16_bf16 v[120:135], v[160:163], v[24:27], v[88:103]\n"
    "v_rcp_f32 v136, v136\n"
    "v_rcp_f32 v137, v137\n"
    "v_rcp_f32 v138, v138\n"
    "v_rcp_f32 v139, v139\n"
    "v_mfma_f32_32x32x16_bf16 v[120:135], v[164:167], v[28:31], v[120:135]\n"
    "v_rcp_f32 v140, v140\n"
    "s_setprio 3\n"
    "ds_read_u16 v176, v11 offset:64\n"
    "ds_read_b64_tr_b16 v[184:185], v7 offset:0\n"
    "ds_read_b64_tr_b16 v[186:187], v8 offset:1024\n"
    "ds_read_b64_tr_b16 v[188:189], v7 offset:2048\n"
    "ds_read_b64_tr_b16 v[190:191], v8 offset:3072\n"
    "s_waitcnt vmcnt(0)\n"
    "ds_read_b64_tr_b16 v[192:193], v9 offset:2048\n"
    "ds_read_b64_tr_b16 v[194:195], v9 offset:2304\n"
    "ds_read_b64_tr_b16 v[196:197], v9 offset:3072\n"
    "ds_read_b64_tr_b16 v[198:199], v9 offset:3328\n"
    "s_setprio 0\n"
    "v_rcp_f32 v141, v141\n"
    "v_rcp_f32 v142, v142\n"
    "v_rcp_f32 v143, v143\n"
    "v_rcp_f32 v144, v144\n"
    "v_rcp_f32 v145, v145\n"
    "v_rcp_f32 v146, v146\n"
    "v_rcp_f32 v147, v147\n"
    "v_rcp_f32 v148, v148\n"
    "v_rcp_f32 v149, v149\n"
    "v_rcp_f32 v150, v150\n"
    "v_rcp_f32 v151, v151\n"
    "v_pk_mul_f32 v[136:137], v[104:105], v[136:137]\n"
    "v_pk_mul_f32 v[138:139], v[106:107], v[138:139]\n"
    "v_pk_mul_f32 v[140:141], v[108:109], v[140:141]\n"
    "v_pk_mul_f32 v[142:143], v[110:111], v[142:143]\n"
    "v_pk_mul_f32 v[144:145], v[112:113], v[144:145]\n"
    "v_pk_mul_f32 v[146:147], v[114:115], v[146:147]\n"
    "v_pk_mul_f32 v[148:149], v[116:117], v[148:149]\n"
    "v_pk_mul_f32 v[150:151], v[118:119], v[150:151]\n"
    "v_pk_fma_f32 v[200:201], v[120:121], v[120:121], s[82:83]\n"
    "v_pk_fma_f32 v[202:203], v[122:123], v[122:123], s[82:83]\n"
    "v_pk_fma_f32 v[204:205], v[124:125], v[124:125], s[82:83]\n"
    "v_pk_fma_f32 v[206:207], v[126:127], v[126:127], s[82:83]\n"
    "v_pk_fma_f32 v[208:209], v[128:129], v[128:129], s[82:83]\n"
    "v_pk_fma_f32 v[210:211], v[130:131], v[130:131], s[82:83]\n"
    "v_pk_fma_f32 v[212:213], v[132:133], v[132:133], s[82:83]\n"
    "v_pk_fma_f32 v[214:215], v[134:135], v[134:135], s[82:83]\n"
    "v_pk_mul_f32 v[200:201], v[120:121], v[200:201] neg_lo:[1,0] neg_hi:[1,0]\n"
    "v_pk_mul_f32 v[202:203], v[122:123], v[202:203] neg_lo:[1,0] neg_hi:[1,0]\n"
    "v_pk_mul_f32 v[204:205], v[124:125], v[204:205] neg_lo:[1,0] neg_hi:[1,0]\n"
    "v_pk_mul_f32 v[206:207], v[126:127], v[206:207] neg_lo:[1,0] neg_hi:[1,0]\n"
    "v_pk_mul_f32 v[208:209], v[128:129], v[208:209] neg_lo:[1,0] neg_hi:[1,0]\n"
    "v_pk_mul_f32 v[210:211], v[130:131], v[210:211] neg_lo:[1,0] neg_hi:[1,0]\n"
    "v_pk_mul_f32 v[212:213], v[132:133], v[212:213] neg_lo:[1,0] neg_hi:[1,0]\n"
    "v_pk_mul_f32 v[214:215], v[134:135], v[214:215] neg_lo:[1,0] neg_hi:[1,0]\n"
    "s_waitcnt lgkmcnt(11)\n"
    "v_smfmac_f32_32x32x32_bf16 v[40:55], v[168:171], v[136:143], v12\n"
    "v_exp_f32 v200, v200\n"
    "v_exp_f32 v201, v201\n"
    "v_exp_f32 v202, v202\n"
    "v_exp_f32 v203, v203\n"
    "s_waitcnt lgkmcnt(9)\n"
    "v_smfmac_f32_32x32x32_bf16 v[40:55], v[172:175], v[144:151], v12\n"
    "v_exp_f32 v204, v204\n"
    "v_exp_f32 v205, v205\n"
    "s_add_u32 m0, s81, 4096\n"
    "v_exp_f32 v206, v206\n"
    "buffer_load_dwordx4 v4, s[44:47], s78 offen lds\n"
    "v_exp_f32 v207, v207\n"
    "v_exp_f32 v208, v208\n"
    "buffer_load_dwordx4 v4, s[44:47], s78 offen offset:1024 lds\n"
    "v_exp_f32 v209, v209\n"
    "v_exp_f32 v210, v210\n"
    "s_mov_b32 m0, s81\n"
    "v_exp_f32 v211, v211\n"
    "s_waitcnt lgkmcnt(4)\n"
    "buffer_load_dwordx4 v2, s[40:43], s76 offen lds\n"
    "v_exp_f32 v212, v212\n"
    "buffer_load_dwordx4 v3, s[40:43], s76 offen offset:1024 lds\n"
    "v_exp_f32 v213, v213\n"
    "v_exp_f32 v214, v214\n"
    "buffer_load_dwordx4 v2, s[40:43], s77 offen offset:2048 lds\n"
    "v_exp_f32 v215, v215\n"
    "buffer_load_dwordx4 v3, s[40:43], s77 offen offset:3072 lds\n"
    "v_pk_fma_f32 v[200:201], v[200:201], s[96:97], s[96:97]\n"
    "v_pk_fma_f32 v[202:203], v[202:203], s[96:97], s[96:97]\n"
    "v_pk_fma_f32 v[204:205], v[204:205], s[96:97], s[96:97]\n"
    "v_pk_fma_f32 v[206:207], v[206:207], s[96:97], s[96:97]\n"
    "v_pk_fma_f32 v[208:209], v[208:209], s[96:97], s[96:97]\n"
    "v_pk_fma_f32 v[210:211], v[210:211], s[96:97], s[96:97]\n"
    "v_pk_fma_f32 v[212:213], v[212:213], s[96:97], s[96:97]\n"
    "v_pk_fma_f32 v[214:215], v[214:215], s[96:97], s[96:97]\n"
    "v_mfma_f32_32x32x16_bf16 v[88:103], v[176:179], v[180:183], 0\n"
    "v_rcp_f32 v200, v200\n"
    "v_rcp_f32 v201, v201\n"
    "v_rcp_f32 v202, v202\n"
    "v_rcp_f32 v203, v203\n"
    "v_mfma_f32_32x32x16_bf16 v[104:119], v[184:187], v[16:19], v[88:103]\n"
    "v_rcp_f32 v204, v204\n"
    "v_rcp_f32 v205, v205\n"
    "v_rcp_f32 v206, v206\n"
    "v_rcp_f32 v207, v207\n"
    "v_mfma_f32_32x32x16_bf16 v[104:119], v[188:191], v[20:23], v[104:119]\n"
    "v_rcp_f32 v208, v208\n"
    "v_rcp_f32 v209, v209\n"
    "v_rcp_f32 v210, v210\n"
    "v_rcp_f32 v211, v211\n"
    "v_rcp_f32 v212, v212\n"
    "v_rcp_f32 v213, v213\n"
    "v_rcp_f32 v214, v214\n"
    "v_rcp_f32 v215, v215\n"
    "v_pk_mul_f32 v[200:201], v[120:121], v[200:201]\n"
    "v_pk_mul_f32 v[202:203], v[122:123], v[202:203]\n"
    "v_pk_mul_f32 v[204:205], v[124:125], v[204:205]\n"
    "v_pk_mul_f32 v[206:207], v[126:127], v[206:207]\n"
    "v_pk_mul_f32 v[208:209], v[128:129], v[208:209]\n"
    "v_pk_mul_f32 v[210:211], v[130:131], v[210:211]\n"
    "v_pk_mul_f32 v[212:213], v[132:133], v[212:213]\n"
    "v_pk_mul_f32 v[214:215], v[134:135], v[214:215]\n"
    "v_pk_fma_f32 v[136:137], v[104:105], v[104:105], s[82:83]\n"
    "v_pk_fma_f32 v[138:139], v[106:107], v[106:107], s[82:83]\n"
    "v_pk_fma_f32 v[140:141], v[108:109], v[108:109], s[82:83]\n"
    "v_pk_fma_f32 v[142:143], v[110:111], v[110:111], s[82:83]\n"
    "v_pk_fma_f32 v[144:145], v[112:113], v[112:113], s[82:83]\n"
    "v_pk_fma_f32 v[146:147], v[114:115], v[114:115], s[82:83]\n"
    "v_pk_fma_f32 v[148:149], v[116:117], v[116:117], s[82:83]\n"
    "v_pk_fma_f32 v[150:151], v[118:119], v[118:119], s[82:83]\n"
    "v_pk_mul_f32 v[136:137], v[104:105], v[136:137] neg_lo:[1,0] neg_hi:[1,0]\n"
    "v_pk_mul_f32 v[138:139], v[106:107], v[138:139] neg_lo:[1,0] neg_hi:[1,0]\n"
    "v_pk_mul_f32 v[140:141], v[108:109], v[140:141] neg_lo:[1,0] neg_hi:[1,0]\n"
    "v_pk_mul_f32 v[142:143], v[110:111], v[142:143] neg_lo:[1,0] neg_hi:[1,0]\n"
    "v_pk_mul_f32 v[144:145], v[112:113], v[144:145] neg_lo:[1,0] neg_hi:[1,0]\n"
    "v_pk_mul_f32 v[146:147], v[114:115], v[146:147] neg_lo:[1,0] neg_hi:[1,0]\n"
    "v_pk_mul_f32 v[148:149], v[116:117], v[148:149] neg_lo:[1,0] neg_hi:[1,0]\n"
    "v_pk_mul_f32 v[150:151], v[118:119], v[150:151] neg_lo:[1,0] neg_hi:[1,0]\n"
    "v_smfmac_f32_32x32x32_bf16 v[56:71], v[168:171], v[200:207], v12\n"
    "v_exp_f32 v136, v136\n"
    "v_exp_f32 v137, v137\n"
    "v_exp_f32 v138, v138\n"
    "v_exp_f32 v139, v139\n"
    "v_smfmac_f32_32x32x32_bf16 v[56:71], v[172:175], v[208:215], v12\n"
    "v_exp_f32 v140, v140\n"
    "v_exp_f32 v141, v141\n"
    "v_add_u32 v11, s85, v10\n"
    "v_exp_f32 v142, v142\n"
    "v_exp_f32 v143, v143\n"
    "v_exp_f32 v144, v144\n"
    "v_exp_f32 v145, v145\n"
    "v_exp_f32 v146, v146\n"
    "v_exp_f32 v147, v147\n"
    "v_exp_f32 v148, v148\n"
    "v_exp_f32 v149, v149\n"
    "v_exp_f32 v150, v150\n"
    "v_exp_f32 v151, v151\n"
    "v_pk_fma_f32 v[136:137], v[136:137], s[96:97], s[96:97]\n"
    "v_pk_fma_f32 v[138:139], v[138:139], s[96:97], s[96:97]\n"
    "v_pk_fma_f32 v[140:141], v[140:141], s[96:97], s[96:97]\n"
    "v_pk_fma_f32 v[142:143], v[142:143], s[96:97], s[96:97]\n"
    "v_pk_fma_f32 v[144:145], v[144:145], s[96:97], s[96:97]\n"
    "v_pk_fma_f32 v[146:147], v[146:147], s[96:97], s[96:97]\n"
    "v_pk_fma_f32 v[148:149], v[148:149], s[96:97], s[96:97]\n"
    "v_pk_fma_f32 v[150:151], v[150:151], s[96:97], s[96:97]\n"
    "v_mfma_f32_32x32x16_bf16 v[120:135], v[184:187], v[24:27], v[88:103]\n"
    "v_rcp_f32 v136, v136\n"
    "v_rcp_f32 v137, v137\n"
    "v_rcp_f32 v138, v138\n"
    "v_rcp_f32 v139, v139\n"
    "v_mfma_f32_32x32x16_bf16 v[120:135], v[188:191], v[28:31], v[120:135]\n"
    "v_rcp_f32 v140, v140\n"
    "s_setprio 3\n"
    "s_waitcnt vmcnt(0)\n"
    "ds_read_u16 v176, v11 offset:0\n"
    "ds_read_b64_tr_b16 v[160:161], v5 offset:0\n"
    "ds_read_b64_tr_b16 v[162:163], v6 offset:1024\n"
    "ds_read_b64_tr_b16 v[164:165], v5 offset:2048\n"
    "ds_read_b64_tr_b16 v[166:167], v6 offset:3072\n"
    "ds_read_b64_tr_b16 v[168:169], v9 offset:0\n"
    "ds_read_b64_tr_b16 v[170:171], v9 offset:256\n"
    "ds_read_b64_tr_b16 v[172:173], v9 offset:1024\n"
    "ds_read_b64_tr_b16 v[174:175], v9 offset:1280\n"
    "s_setprio 0\n"
    "v_rcp_f32 v141, v141\n"
    "v_rcp_f32 v142, v142\n"
    "v_rcp_f32 v143, v143\n"
    "v_rcp_f32 v144, v144\n"
    "v_rcp_f32 v145, v145\n"
    "v_rcp_f32 v146, v146\n"
    "v_rcp_f32 v147, v147\n"
    "v_rcp_f32 v148, v148\n"
    "v_rcp_f32 v149, v149\n"
    "v_rcp_f32 v150, v150\n"
    "v_rcp_f32 v151, v151\n"
    "v_pk_mul_f32 v[136:137], v[104:105], v[136:137]\n"
    "v_pk_mul_f32 v[138:139], v[106:107], v[138:139]\n"
    "v_pk_mul_f32 v[140:141], v[108:109], v[140:141]\n"
    "v_pk_mul_f32 v[142:143], v[110:111], v[142:143]\n"
    "v_pk_mul_f32 v[144:145], v[112:113], v[144:145]\n"
    "v_pk_mul_f32 v[146:147], v[114:115], v[146:147]\n"
    "v_pk_mul_f32 v[148:149], v[116:117], v[148:149]\n"
    "v_pk_mul_f32 v[150:151], v[118:119], v[150:151]\n"
    "v_pk_fma_f32 v[200:201], v[120:121], v[120:121], s[82:83]\n"
    "v_pk_fma_f32 v[202:203], v[122:123], v[122:123], s[82:83]\n"
    "v_pk_fma_f32 v[204:205], v[124:125], v[124:125], s[82:83]\n"
    "v_pk_fma_f32 v[206:207], v[126:127], v[126:127], s[82:83]\n"
    "v_pk_fma_f32 v[208:209], v[128:129], v[128:129], s[82:83]\n"
    "v_pk_fma_f32 v[210:211], v[130:131], v[130:131], s[82:83]\n"
    "v_pk_fma_f32 v[212:213], v[132:133], v[132:133], s[82:83]\n"
    "v_pk_fma_f32 v[214:215], v[134:135], v[134:135], s[82:83]\n"
    "v_pk_mul_f32 v[200:201], v[120:121], v[200:201] neg_lo:[1,0] neg_hi:[1,0]\n"
    "v_pk_mul_f32 v[202:203], v[122:123], v[202:203] neg_lo:[1,0] neg_hi:[1,0]\n"
    "v_pk_mul_f32 v[204:205], v[124:125], v[204:205] neg_lo:[1,0] neg_hi:[1,0]\n"
    "v_pk_mul_f32 v[206:207], v[126:127], v[206:207] neg_lo:[1,0] neg_hi:[1,0]\n"
    "v_pk_mul_f32 v[208:209], v[128:129], v[208:209] neg_lo:[1,0] neg_hi:[1,0]\n"
    "v_pk_mul_f32 v[210:211], v[130:131], v[210:211] neg_lo:[1,0] neg_hi:[1,0]\n"
    "v_pk_mul_f32 v[212:213], v[132:133], v[212:213] neg_lo:[1,0] neg_hi:[1,0]\n"
    "v_pk_mul_f32 v[214:215], v[134:135], v[214:215] neg_lo:[1,0] neg_hi:[1,0]\n"
    "s_waitcnt lgkmcnt(11)\n"
    "v_smfmac_f32_32x32x32_bf16 v[40:55], v[192:195], v[136:143], v12\n"
    "v_exp_f32 v200, v200\n"
    "v_exp_f32 v201, v201\n"
    "v_exp_f32 v202, v202\n"
    "v_exp_f32 v203, v203\n"
    "s_waitcnt lgkmcnt(9)\n"
    "v_smfmac_f32_32x32x32_bf16 v[40:55], v[196:199], v[144:151], v12\n"
    "v_exp_f32 v204, v204\n"
    "v_exp_f32 v205, v205\n"
    "s_add_u32 m0, s81, 6144\n"
    "v_exp_f32 v206, v206\n"
    "buffer_load_dwordx4 v4, s[44:47], s79 offen lds\n"
    "v_exp_f32 v207, v207\n"
    "v_exp_f32 v208, v208\n"
    "buffer_load_dwordx4 v4, s[44:47], s79 offen offset:1024 lds\n"
    "v_exp_f32 v209, v209\n"
    "v_exp_f32 v210, v210\n"
    "v_exp_f32 v211, v211\n"
    "v_exp_f32 v212, v212\n"
    "v_exp_f32 v213, v213\n"
    "v_exp_f32 v214, v214\n"
    "v_exp_f32 v215, v215\n"
    "v_pk_fma_f32 v[200:201], v[200:201], s[96:97], s[96:97]\n"
    "v_pk_fma_f32 v[202:203], v[202:203], s[96:97], s[96:97]\n"
    "v_pk_fma_f32 v[204:205], v[204:205], s[96:97], s[96:97]\n"
    "v_pk_fma_f32 v[206:207], v[206:207], s[96:97], s[96:97]\n"
    "v_pk_fma_f32 v[208:209], v[208:209], s[96:97], s[96:97]\n"
    "v_pk_fma_f32 v[210:211], v[210:211], s[96:97], s[96:97]\n"
    "v_pk_fma_f32 v[212:213], v[212:213], s[96:97], s[96:97]\n"
    "v_pk_fma_f32 v[214:215], v[214:215], s[96:97], s[96:97]\n"
    "s_waitcnt lgkmcnt(8)\n"
    "v_mfma_f32_32x32x16_bf16 v[88:103], v[176:179], v[180:183], 0\n"
    "v_rcp_f32 v200, v200\n"
    "v_rcp_f32 v201, v201\n"
    "v_rcp_f32 v202, v202\n"
    "v_rcp_f32 v203, v203\n"
    "s_waitcnt lgkmcnt(6)\n"
    "v_mfma_f32_32x32x16_bf16 v[104:119], v[160:163], v[16:19], v[88:103]\n"
    "v_rcp_f32 v204, v204\n"
    "v_rcp_f32 v205, v205\n"
    "v_rcp_f32 v206, v206\n"
    "v_rcp_f32 v207, v207\n"
    "s_waitcnt lgkmcnt(4)\n"
    "v_mfma_f32_32x32x16_bf16 v[104:119], v[164:167], v[20:23], v[104:119]\n"
    "v_rcp_f32 v208, v208\n"
    "v_rcp_f32 v209, v209\n"
    "v_rcp_f32 v210, v210\n"
    "v_rcp_f32 v211, v211\n"
    "v_rcp_f32 v212, v212\n"
    "v_rcp_f32 v213, v213\n"
    "v_rcp_f32 v214, v214\n"
    "v_rcp_f32 v215, v215\n"
    "v_pk_mul_f32 v[200:201], v[120:121], v[200:201]\n"
    "v_pk_mul_f32 v[202:203], v[122:123], v[202:203]\n"
    "v_pk_mul_f32 v[204:205], v[124:125], v[204:205]\n"
    "v_pk_mul_f32 v[206:207], v[126:127], v[206:207]\n"
    "v_pk_mul_f32 v[208:209], v[128:129], v[208:209]\n"
    "v_pk_mul_f32 v[210:211], v[130:131], v[210:211]\n"
    "v_pk_mul_f32 v[212:213], v[132:133], v[212:213]\n"
    "v_pk_mul_f32 v[214:215], v[134:135], v[214:215]\n"
    ".Lmx_pfv:\n"
    "s_cmp_eq_u32 s93, 1\n"
    "s_cbranch_scc0 .Lmx_npv\n"
    "s_lshl_b32 s88, s73, 11\n"
    "s_lshl_b32 s86, s69, 8\n"
    "s_add_u32 s88, s88, s86\n"
    "s_add_u32 s88, s88, 1048576\n"
    "s_add_u32 s88, s88, s37\n"
    "v_mov_b32 v13, s80\n"
    "s_mov_b64 exec, 1\n"
    "buffer_store_dword v13, off, s[56:59], s88 sc1\n"
    "s_mov_b64 exec, -1\n"
    "s_mov_b32 s93, 0\n"
    ".Lmx_npv:\n"
    "s_cmp_lg_u32 s36, 0\n"
    "s_cbranch_scc1 .Lmx_bv\n"
    ".Lmx_rv:\n"
    "s_sub_u32 s84, s84, 1\n"
    "s_cmp_lg_u32 s84, 0\n"
    "s_cbranch_scc1 .Lmx_p2\n"
    "s_cmp_eq_u32 s36, 1\n"
    "s_cselect_b32 s84, 1, s93\n"
    "s_cmp_eq_u32 s84, 0\n"
    "s_cbranch_scc1 .Lmx_okv\n"
    "s_add_u32 s39, s39, 1\n"
    "s_cmp_lt_u32 s39, 0x400000\n"
    "s_cbranch_scc0 .Lmx_okv\n"
    "s_waitcnt vmcnt(0)\n"
    "s_branch .Lmx_pfv\n"
    ".Lmx_okv:\n"
    "v_smfmac_f32_32x32x32_bf16 v[56:71], v[192:195], v[200:207], v12\n"
    "v_smfmac_f32_32x32x32_bf16 v[56:71], v[196:199], v[208:215], v12\n"
    "s_branch .Lmx_epi\n"
    ".Lmx_bv:\n"
    "s_cmp_eq_u32 s36, 1\n"
    "s_cbranch_scc0 .Lmx_bf\n"
    "v_mov_b32 v13, 162820\n"
    "ds_read_b32 v14, v13\n"
    "s_waitcnt lgkmcnt(0)\n"
    "v_readfirstlane_b32 s89, v14\n"
    "s_cmp_eq_u32 s89, 3\n"
    "s_cbranch_scc0 .Lmx_rv\n"
    "v_lshlrev_b32 v15, 4, v1\n"
    "v_add_u32 v15, 140288, v15\n"
    "ds_read_b128 v[216:219], v15 offset:12288\n"
    "ds_read_b128 v[220:223], v15 offset:13312\n"
    "ds_read_b128 v[224:227], v15 offset:14336\n"
    "ds_read_b128 v[228:231], v15 offset:15360\n"
    "ds_read_b128 v[152:155], v15 offset:16384\n"
    "ds_read_b128 v[156:159], v15 offset:17408\n"
    "ds_read_b128 v[242:245], v15 offset:18432\n"
    "ds_read_b128 v[246:249], v15 offset:19456\n"
    "s_waitcnt lgkmcnt(7)\n"
    "v_pk_add_f32 v[72:73], v[72:73], v[216:217]\n"
    "v_pk_add_f32 v[74:75], v[74:75], v[218:219]\n"
    "s_waitcnt lgkmcnt(6)\n"
    "v_pk_add_f32 v[76:77], v[76:77], v[220:221]\n"
    "v_pk_add_f32 v[78:79], v[78:79], v[222:223]\n"
    "s_waitcnt lgkmcnt(5)\n"
    "v_pk_add_f32 v[80:81], v[80:81], v[224:225]\n"
    "v_pk_add_f32 v[82:83], v[82:83], v[226:227]\n"
    "s_waitcnt lgkmcnt(4)\n"
    "v_pk_add_f32 v[84:85], v[84:85], v[228:229]\n"
    "v_pk_add_f32 v[86:87], v[86:87], v[230:231]\n"
    "ds_read_b128 v[216:219], v15 offset:0\n"
    "ds_read_b128 v[220:223], v15 offset:1024\n"
    "ds_read_b128 v[224:227], v15 offset:2048\n"
    "ds_read_b128 v[228:231], v15 offset:3072\n"
    "s_waitcnt lgkmcnt(7)\n"
    "v_pk_add_f32 v[72:73], v[72:73], v[152:153]\n"
    "v_pk_add_f32 v[74:75], v[74:75], v[154:155]\n"
    "s_waitcnt lgkmcnt(6)\n"
    "v_pk_add_f32 v[76:77], v[76:77], v[156:157]\n"
    "v_pk_add_f32 v[78:79], v[78:79], v[158:159]\n"
    "s_waitcnt lgkmcnt(5)\n"
    "v_pk_add_f32 v[80:81], v[80:81], v[242:243]\n"
    "v_pk_add_f32 v[82:83], v[82:83], v[244:245]\n"
    "s_waitcnt lgkmcnt(4)\n"
    "v_pk_add_f32 v[84:85], v[84:85], v[246:247]\n"
    "v_pk_add_f32 v[86:87], v[86:87], v[248:249]\n"
    "s_waitcnt lgkmcnt(3)\n"
    "v_pk_add_f32 v[72:73], v[72:73], v[216:217]\n"
    "v_pk_add_f32 v[74:75], v[74:75], v[218:219]\n"
    "s_waitcnt lgkmcnt(2)\n"
    "v_pk_add_f32 v[76:77], v[76:77], v[220:221]\n"
    "v_pk_add_f32 v[78:79], v[78:79], v[222:223]\n"
    "s_waitcnt lgkmcnt(1)\n"
    "v_pk_add_f32 v[80:81], v[80:81], v[224:225]\n"
    "v_pk_add_f32 v[82:83], v[82:83], v[226:227]\n"
    "s_waitcnt lgkmcnt(0)\n"
    "v_pk_add_f32 v[84:85], v[84:85], v[228:229]\n"
    "v_pk_add_f32 v[86:87], v[86:87], v[230:231]\n"
    "s_lshl_b32 s88, s73, 14\n"
    "s_lshl_b32 s86, s69, 12\n"
    "s_add_u32 s88, s88, s86\n"
    "buffer_store_dwordx4 v[72:75], v4, s[56:59], s88 offen offset:0 sc1\n"
    "buffer_store_dwordx4 v[76:79], v4, s[56:59], s88 offen offset:1024 sc1\n"
    "buffer_store_dwordx4 v[80:83], v4, s[56:59], s88 offen offset:2048 sc1\n"
    "buffer_store_dwordx4 v[84:87], v4, s[56:59], s88 offen offset:3072 sc1\n"
    "s_mov_b32 s93, 1\n"
    "v_and_b32 v242, 3, v1\n"
    "v_lshlrev_b32 v242, 8, v242\n"
    "v_bfe_u32 v245, v1, 2, 1\n"
    "v_lshl_add_u32 v242, v245, 7, v242\n"
    "s_lshl_b32 s88, s73, 11\n"
    "s_add_u32 s88, s88, 1048576\n"
    "v_add_u32 v242, s88, v242\n"
    "v_lshrrev_b32 v244, 4, v1\n"
    "v_lshlrev_b32 v244, 10, v244\n"
    "v_bfe_u32 v245, v1, 3, 1\n"
    "v_lshl_add_u32 v244, v245, 9, v244\n"
    "v_and_b32 v245, 7, v1\n"
    "v_lshl_add_u32 v244, v245, 4, v244\n"
    "s_lshl_b32 s86, s69, 7\n"
    "s_lshl_b32 s88, s73, 14\n"
    "s_add_u32 s86, s86, s88\n"
    "v_add_u32 v244, s86, v244\n"
    "s_mov_b32 s36, 2\n"
    "s_branch .Lmx_rv\n"
    ".Lmx_bf:\n"
    "s_cmp_eq_u32 s36, 2\n"
    "s_cbranch_scc1 .Lmx_fi\n"
    "v_cmp_ne_u32 vcc, s80, v243\n"
    "s_nop 4\n"
    "s_cbranch_vccz .Lmx_fg\n"
    ".Lmx_fi:\n"
    "s_cmp_gt_u32 s84, 3\n"
    "s_cbranch_scc1 .Lmx_rv\n"
    "buffer_load_dword v243, v242, s[56:59], 0 offen sc1\n"
    "s_mov_b32 s36, 3\n"
    "s_branch .Lmx_rv\n"
    ".Lmx_fg:\n"
    "buffer_load_dwordx4 v[216:219], v244, s[56:59], 0 offen sc1\n"
    "s_mov_b32 s86, 4096\n"
    "buffer_load_dwordx4 v[220:223], v244, s[56:59], s86 offen sc1\n"
    "s_mov_b32 s86, 8192\n"
    "buffer_load_dwordx4 v[224:227], v244, s[56:59], s86 offen sc1\n"
    "s_mov_b32 s86, 12288\n"
    "buffer_load_dwordx4 v[228:231], v244, s[56:59], s86 offen sc1\n"
    "s_mov_b32 s86, 2097152\n"
    "buffer_load_dwordx4 v[72:75], v244, s[56:59], s86 offen sc1\n"
    "s_mov_b32 s86, 2101248\n"
    "buffer_load_dwordx4 v[76:79], v244, s[56:59], s86 offen sc1\n"
    "s_mov_b32 s86, 2105344\n"
    "buffer_load_dwordx4 v[80:83], v244, s[56:59], s86 offen sc1\n"
    "s_mov_b32 s86, 2109440\n"
    "buffer_load_dwordx4 v[84:87], v244, s[56:59], s86 offen sc1\n"
    "s_cmp_lg_u32 s69, 0\n"
    "s_cbranch_scc1 .Lmx_nepa\n"
    "s_lshl_b32 s86, s73, 11\n"
    "s_add_u32 s86, s86, 1049600\n"
    "v_mov_b32 v13, s80\n"
    "s_mov_b64 exec, 1\n"
    "buffer_store_dword v13, off, s[56:59], s86 sc1\n"
    "s_mov_b64 exec, -1\n"
    ".Lmx_nepa:\n"
    "s_mov_b32 s36, 0\n"
    "s_mov_b32 s38, 1\n"
    "s_branch .Lmx_rv\n"
    ".Lmx_bl:\n"
    "v_mov_b32 v13, 162816\n"
    "ds_read_b32 v14, v13\n"
    "s_waitcnt lgkmcnt(0)\n"
    "v_readfirstlane_b32 s89, v14\n"
    "s_cmp_eq_u32 s89, 3\n"
    "s_cbranch_scc0 .Lmx_rl\n"
    "v_lshlrev_b32 v15, 4, v1\n"
    "v_add_u32 v15, 140288, v15\n"
    "ds_read_b128 v[208:211], v15 offset:0\n"
    "ds_read_b128 v[212:215], v15 offset:1024\n"
    "ds_read_b128 v[216:219], v15 offset:2048\n"
    "ds_read_b128 v[220:223], v15 offset:3072\n"
    "ds_read_b128 v[224:227], v15 offset:4096\n"
    "ds_read_b128 v[228:231], v15 offset:5120\n"
    "ds_read_b128 v[242:245], v15 offset:6144\n"
    "ds_read_b128 v[246:249], v15 offset:7168\n"
    "s_waitcnt lgkmcnt(7)\n"
    "v_pk_add_f32 v[72:73], v[72:73], v[208:209]\n"
    "v_pk_add_f32 v[74:75], v[74:75], v[210:211]\n"
    "s_waitcnt lgkmcnt(6)\n"
    "v_pk_add_f32 v[76:77], v[76:77], v[212:213]\n"
    "v_pk_add_f32 v[78:79], v[78:79], v[214:215]\n"
    "s_waitcnt lgkmcnt(5)\n"
    "v_pk_add_f32 v[80:81], v[80:81], v[216:217]\n"
    "v_pk_add_f32 v[82:83], v[82:83], v[218:219]\n"
    "s_waitcnt lgkmcnt(4)\n"
    "v_pk_add_f32 v[84:85], v[84:85], v[220:221]\n"
    "v_pk_add_f32 v[86:87], v[86:87], v[222:223]\n"
    "ds_read_b128 v[208:211], v15 offset:8192\n"
    "ds_read_b128 v[212:215], v15 offset:9216\n"
    "ds_read_b128 v[216:219], v15 offset:10240\n"
    "ds_read_b128 v[220:223], v15 offset:11264\n"
    "s_waitcnt lgkmcnt(7)\n"
    "v_pk_add_f32 v[72:73], v[72:73], v[224:225]\n"
    "v_pk_add_f32 v[74:75], v[74:75], v[226:227]\n"
    "s_waitcnt lgkmcnt(6)\n"
    "v_pk_add_f32 v[76:77], v[76:77], v[228:229]\n"
    "v_pk_add_f32 v[78:79], v[78:79], v[230:231]\n"
    "s_waitcnt lgkmcnt(5)\n"
    "v_pk_add_f32 v[80:81], v[80:81], v[242:243]\n"
    "v_pk_add_f32 v[82:83], v[82:83], v[244:245]\n"
    "s_waitcnt lgkmcnt(4)\n"
    "v_pk_add_f32 v[84:85], v[84:85], v[246:247]\n"
    "v_pk_add_f32 v[86:87], v[86:87], v[248:249]\n"
    "s_waitcnt lgkmcnt(3)\n"
    "v_pk_add_f32 v[72:73], v[72:73], v[208:209]\n"
    "v_pk_add_f32 v[74:75], v[74:75], v[210:211]\n"
    "s_waitcnt lgkmcnt(2)\n"
    "v_pk_add_f32 v[76:77], v[76:77], v[212:213]\n"
    "v_pk_add_f32 v[78:79], v[78:79], v[214:215]\n"
    "s_waitcnt lgkmcnt(1)\n"
    "v_pk_add_f32 v[80:81], v[80:81], v[216:217]\n"
    "v_pk_add_f32 v[82:83], v[82:83], v[218:219]\n"
    "s_waitcnt lgkmcnt(0)\n"
    "v_pk_add_f32 v[84:85], v[84:85], v[220:221]\n"
    "v_pk_add_f32 v[86:87], v[86:87], v[222:223]\n"
    "ds_write_b128 v15, v[72:75] offset:0\n"
    "ds_write_b128 v15, v[76:79] offset:1024\n"
    "ds_write_b128 v15, v[80:83] offset:2048\n"
    "ds_write_b128 v15, v[84:87] offset:3072\n"
    "v_mov_b32 v13, 162820\n"
    "v_mov_b32 v14, 1\n"
    "s_waitcnt lgkmcnt(0)\n"
    "s_mov_b64 exec, 1\n"
    "ds_add_u32 v13, v14\n"
    "s_mov_b64 exec, -1\n"
    "s_mov_b32 s36, 0\n"
    "s_branch .Lmx_rl\n"
    ".Lmx_L:\n"
    "s_setprio 2\n"
    ".p2align 6\n"
    ".Lmx_q1:\n"
    "s_waitcnt vmcnt(7)\n"
    "ds_read_b64_tr_b16 v[168:169], v9 offset:0\n"
    "ds_read_b64_tr_b16 v[170:171], v9 offset:256\n"
    "ds_read_b64_tr_b16 v[172:173], v9 offset:1024\n"
    "ds_read_b64_tr_b16 v[174:175], v9 offset:1280\n"
    "v_mfma_f32_32x32x16_bf16 v[120:135], v[160:163], v[24:27], v[88:103]\n"
    "v_lshlrev_b32_sdwa v136, 1, v104 dst_sel:DWORD dst_unused:UNUSED_PAD src0_sel:DWORD src1_sel:WORD_1\n"
    "s_waitcnt lgkmcnt(3)\n"
    "ds_read_u16 v136, v136\n"
    "v_lshlrev_b32_sdwa v137, 1, v105 dst_sel:DWORD dst_unused:UNUSED_PAD src0_sel:DWORD src1_sel:WORD_1\n"
    "s_waitcnt lgkmcnt(3)\n"
    "ds_read_u16 v137, v137\n"
    "v_lshlrev_b32_sdwa v138, 1, v106 dst_sel:DWORD dst_unused:UNUSED_PAD src0_sel:DWORD src1_sel:WORD_1\n"
    "s_waitcnt lgkmcnt(3)\n"
    "ds_read_u16 v138, v138\n"
    "v_lshlrev_b32_sdwa v139, 1, v107 dst_sel:DWORD dst_unused:UNUSED_PAD src0_sel:DWORD src1_sel:WORD_1\n"
    "s_waitcnt lgkmcnt(3)\n"
    "ds_read_u16 v139, v139\n"
    "v_smfmac_f32_32x32x32_bf16 v[72:87], v[200:203], v[184:191], v12\n"
    "v_lshlrev_b32_sdwa v140, 1, v108 dst_sel:DWORD dst_unused:UNUSED_PAD src0_sel:DWORD src1_sel:WORD_1\n"
    "s_waitcnt lgkmcnt(3)\n"
    "ds_read_u16 v140, v140\n"
    "s_add_u32 s74, s74, 1\n"
    "s_cmp_eq_u32 s74, 11\n"
    "s_cselect_b32 s74, 0, s74\n"
    "s_add_u32 s86, s92, s74\n"
    "s_lshl_b32 s76, s86, 7\n"
    "v_lshlrev_b32_sdwa v141, 1, v109 dst_sel:DWORD dst_unused:UNUSED_PAD src0_sel:DWORD src1_sel:WORD_1\n"
    "s_waitcnt lgkmcnt(3)\n"
    "ds_read_u16 v141, v141\n"
    "ds_read_b64_tr_b16 v[152:153], v7 offset:0\n"
    "ds_read_b64_tr_b16 v[154:155], v8 offset:1024\n"
    "ds_read_b64_tr_b16 v[156:157], v7 offset:2048\n"
    "ds_read_b64_tr_b16 v[158:159], v8 offset:3072\n"
    "v_lshlrev_b32_sdwa v142, 1, v110 dst_sel:DWORD dst_unused:UNUSED_PAD src0_sel:DWORD src1_sel:WORD_1\n"
    "s_waitcnt lgkmcnt(3)\n"
    "ds_read_u16 v142, v142\n"
    "v_lshlrev_b32_sdwa v143, 1, v111 dst_sel:DWORD dst_unused:UNUSED_PAD src0_sel:DWORD src1_sel:WORD_1\n"
    "s_waitcnt lgkmcnt(3)\n"
    "ds_read_u16 v143, v143\n"
    "v_mfma_f32_32x32x16_bf16 v[120:135], v[164:167], v[28:31], v[120:135]\n"
    "v_lshlrev_b32_sdwa v144, 1, v112 dst_sel:DWORD dst_unused:UNUSED_PAD src0_sel:DWORD src1_sel:WORD_1\n"
    "s_waitcnt lgkmcnt(3)\n"
    "ds_read_u16 v144, v144\n"
    "s_add_u32 s77, s76, 145408\n"
    "s_lshl_b32 s78, s86, 12\n"
    "s_add_u32 s79, s78, 2048\n"
    "s_lshl_b32 s85, s74, 7\n"
    "v_lshlrev_b32_sdwa v145, 1, v113 dst_sel:DWORD dst_unused:UNUSED_PAD src0_sel:DWORD src1_sel:WORD_1\n"
    "s_waitcnt lgkmcnt(3)\n"
    "ds_read_u16 v145, v145\n"
    "ds_read_u16 v176, v11 offset:64\n"
    "v_lshlrev_b32_sdwa v146, 1, v114 dst_sel:DWORD dst_unused:UNUSED_PAD src0_sel:DWORD src1_sel:WORD_1\n"
    "s_waitcnt lgkmcnt(3)\n"
    "ds_read_u16 v146, v146\n"
    "v_lshlrev_b32_sdwa v147, 1, v115 dst_sel:DWORD dst_unused:UNUSED_PAD src0_sel:DWORD src1_sel:WORD_1\n"
    "s_waitcnt lgkmcnt(3)\n"
    "ds_read_u16 v147, v147\n"
    "v_smfmac_f32_32x32x32_bf16 v[72:87], v[204:207], v[192:199], v12\n"
    "v_lshlrev_b32_sdwa v148, 1, v116 dst_sel:DWORD dst_unused:UNUSED_PAD src0_sel:DWORD src1_sel:WORD_1\n"
    "s_waitcnt lgkmcnt(3)\n"
    "ds_read_u16 v148, v148\n"
    "v_lshlrev_b32_sdwa v149, 1, v117 dst_sel:DWORD dst_unused:UNUSED_PAD src0_sel:DWORD src1_sel:WORD_1\n"
    "s_waitcnt lgkmcnt(3)\n"
    "ds_read_u16 v149, v149\n"
    "v_lshlrev_b32_sdwa v150, 1, v118 dst_sel:DWORD dst_unused:UNUSED_PAD src0_sel:DWORD src1_sel:WORD_1\n"
    "s_waitcnt lgkmcnt(3)\n"
    "ds_read_u16 v150, v150\n"
    "v_lshlrev_b32_sdwa v151, 1, v119 dst_sel:DWORD dst_unused:UNUSED_PAD src0_sel:DWORD src1_sel:WORD_1\n"
    "s_waitcnt lgkmcnt(3)\n"
    "ds_read_u16 v151, v151\n"
    "v_mfma_f32_32x32x16_bf16 v[104:119], v[160:163], v[32:35], v[88:103]\n"
    "v_lshlrev_b32_sdwa v184, 1, v120 dst_sel:DWORD dst_unused:UNUSED_PAD src0_sel:DWORD src1_sel:WORD_1\n"
    "s_waitcnt lgkmcnt(3)\n"
    "ds_read_u16 v184, v184\n"
    "s_add_u32 m0, s81, 4096\n"
    "s_nop 0\n"
    "buffer_load_dwordx4 v4, s[44:47], s78 offen lds\n"
    "v_lshlrev_b32_sdwa v185, 1, v121 dst_sel:DWORD dst_unused:UNUSED_PAD src0_sel:DWORD src1_sel:WORD_1\n"
    "s_waitcnt lgkmcnt(3)\n"
    "ds_read_u16 v185, v185\n"
    "v_lshlrev_b32_sdwa v186, 1, v122 dst_sel:DWORD dst_unused:UNUSED_PAD src0_sel:DWORD src1_sel:WORD_1\n"
    "s_waitcnt lgkmcnt(3)\n"
    "ds_read_u16 v186, v186\n"
    "v_lshlrev_b32_sdwa v187, 1, v123 dst_sel:DWORD dst_unused:UNUSED_PAD src0_sel:DWORD src1_sel:WORD_1\n"
    "s_waitcnt lgkmcnt(3)\n"
    "ds_read_u16 v187, v187\n"
    "v_smfmac_f32_32x32x32_bf16 v[40:55], v[168:171], v[136:143], v12\n"
    "v_lshlrev_b32_sdwa v188, 1, v124 dst_sel:DWORD dst_unused:UNUSED_PAD src0_sel:DWORD src1_sel:WORD_1\n"
    "s_waitcnt lgkmcnt(3)\n"
    "ds_read_u16 v188, v188\n"
    "buffer_load_dwordx4 v4, s[44:47], s78 offen offset:1024 lds\n"
    "v_lshlrev_b32_sdwa v189, 1, v125 dst_sel:DWORD dst_unused:UNUSED_PAD src0_sel:DWORD src1_sel:WORD_1\n"
    "s_waitcnt lgkmcnt(3)\n"
    "ds_read_u16 v189, v189\n"
    "v_lshlrev_b32_sdwa v190, 1, v126 dst_sel:DWORD dst_unused:UNUSED_PAD src0_sel:DWORD src1_sel:WORD_1\n"
    "s_waitcnt lgkmcnt(3)\n"
    "ds_read_u16 v190, v190\n"
    "v_lshlrev_b32_sdwa v191, 1, v127 dst_sel:DWORD dst_unused:UNUSED_PAD src0_sel:DWORD src1_sel:WORD_1\n"
    "s_waitcnt lgkmcnt(3)\n"
    "ds_read_u16 v191, v191\n"
    "v_mfma_f32_32x32x16_bf16 v[104:119], v[164:167], v[36:39], v[104:119]\n"
    "v_lshlrev_b32_sdwa v192, 1, v128 dst_sel:DWORD dst_unused:UNUSED_PAD src0_sel:DWORD src1_sel:WORD_1\n"
    "s_waitcnt lgkmcnt(3)\n"
    "ds_read_u16 v192, v192\n"
    "s_mov_b32 m0, s81\n"
    "s_nop 0\n"
    "buffer_load_dwordx4 v2, s[40:43], s76 offen lds\n"
    "buffer_load_dwordx4 v3, s[40:43], s76 offen offset:1024 lds\n"
    "v_lshlrev_b32_sdwa v193, 1, v129 dst_sel:DWORD dst_unused:UNUSED_PAD src0_sel:DWORD src1_sel:WORD_1\n"
    "s_waitcnt lgkmcnt(3)\n"
    "ds_read_u16 v193, v193\n"
    "v_lshlrev_b32_sdwa v194, 1, v130 dst_sel:DWORD dst_unused:UNUSED_PAD src0_sel:DWORD src1_sel:WORD_1\n"
    "s_waitcnt lgkmcnt(3)\n"
    "ds_read_u16 v194, v194\n"
    "v_lshlrev_b32_sdwa v195, 1, v131 dst_sel:DWORD dst_unused:UNUSED_PAD src0_sel:DWORD src1_sel:WORD_1\n"
    "s_waitcnt lgkmcnt(3)\n"
    "ds_read_u16 v195, v195\n"
    "v_smfmac_f32_32x32x32_bf16 v[40:55], v[172:175], v[144:151], v12\n"
    "v_lshlrev_b32_sdwa v196, 1, v132 dst_sel:DWORD dst_unused:UNUSED_PAD src0_sel:DWORD src1_sel:WORD_1\n"
    "s_waitcnt lgkmcnt(3)\n"
    "ds_read_u16 v196, v196\n"
    "buffer_load_dwordx4 v2, s[40:43], s77 offen offset:2048 lds\n"
    "buffer_load_dwordx4 v3, s[40:43], s77 offen offset:3072 lds\n"
    "v_lshlrev_b32_sdwa v197, 1, v133 dst_sel:DWORD dst_unused:UNUSED_PAD src0_sel:DWORD src1_sel:WORD_1\n"
    "s_waitcnt lgkmcnt(3)\n"
    "ds_read_u16 v197, v197\n"
    "v_lshlrev_b32_sdwa v198, 1, v134 dst_sel:DWORD dst_unused:UNUSED_PAD src0_sel:DWORD src1_sel:WORD_1\n"
    "s_waitcnt lgkmcnt(3)\n"
    "ds_read_u16 v198, v198\n"
    "v_lshlrev_b32_sdwa v199, 1, v135 dst_sel:DWORD dst_unused:UNUSED_PAD src0_sel:DWORD src1_sel:WORD_1\n"
    "s_waitcnt lgkmcnt(3)\n"
    "ds_read_u16 v199, v199\n"
    "v_lshlrev_b32_sdwa v136, 1, v104 dst_sel:DWORD dst_unused:UNUSED_PAD src0_sel:DWORD src1_sel:WORD_1\n"
    "s_waitcnt lgkmcnt(3)\n"
    "ds_read_u16 v136, v136\n"
    "v_lshlrev_b32_sdwa v137, 1, v105 dst_sel:DWORD dst_unused:UNUSED_PAD src0_sel:DWORD src1_sel:WORD_1\n"
    "s_waitcnt lgkmcnt(3)\n"
    "ds_read_u16 v137, v137\n"
    "v_mfma_f32_32x32x16_bf16 v[88:103], v[176:179], v[180:183], 0\n"
    "v_lshlrev_b32_sdwa v138, 1, v106 dst_sel:DWORD dst_unused:UNUSED_PAD src0_sel:DWORD src1_sel:WORD_1\n"
    "s_waitcnt lgkmcnt(3)\n"
    "ds_read_u16 v138, v138\n"
    "v_lshlrev_b32_sdwa v139, 1, v107 dst_sel:DWORD dst_unused:UNUSED_PAD src0_sel:DWORD src1_sel:WORD_1\n"
    "s_waitcnt lgkmcnt(3)\n"
    "ds_read_u16 v139, v139\n"
    "v_smfmac_f32_32x32x32_bf16 v[56:71], v[168:171], v[184:191], v12\n"
    "v_lshlrev_b32_sdwa v140, 1, v108 dst_sel:DWORD dst_unused:UNUSED_PAD src0_sel:DWORD src1_sel:WORD_1\n"
    "s_waitcnt lgkmcnt(3)\n"
    "ds_read_u16 v140, v140\n"
    "v_lshlrev_b32_sdwa v141, 1, v109 dst_sel:DWORD dst_unused:UNUSED_PAD src0_sel:DWORD src1_sel:WORD_1\n"
    "s_waitcnt lgkmcnt(3)\n"
    "ds_read_u16 v141, v141\n"
    "v_mfma_f32_32x32x16_bf16 v[120:135], v[152:155], v[16:19], v[88:103]\n"
    "v_lshlrev_b32_sdwa v142, 1, v110 dst_sel:DWORD dst_unused:UNUSED_PAD src0_sel:DWORD src1_sel:WORD_1\n"
    "s_waitcnt lgkmcnt(3)\n"
    "ds_read_u16 v142, v142\n"
    "v_lshlrev_b32_sdwa v143, 1, v111 dst_sel:DWORD dst_unused:UNUSED_PAD src0_sel:DWORD src1_sel:WORD_1\n"
    "s_waitcnt lgkmcnt(3)\n"
    "ds_read_u16 v143, v143\n"
    "v_lshlrev_b32_sdwa v144, 1, v112 dst_sel:DWORD dst_unused:UNUSED_PAD src0_sel:DWORD src1_sel:WORD_1\n"
    "s_waitcnt lgkmcnt(3)\n"
    "ds_read_u16 v144, v144\n"
    "v_lshlrev_b32_sdwa v145, 1, v113 dst_sel:DWORD dst_unused:UNUSED_PAD src0_sel:DWORD src1_sel:WORD_1\n"
    "s_waitcnt lgkmcnt(3)\n"
    "ds_read_u16 v145, v145\n"
    "v_mfma_f32_32x32x16_bf16 v[120:135], v[156:159], v[20:23], v[120:135]\n"
    "v_lshlrev_b32_sdwa v146, 1, v114 dst_sel:DWORD dst_unused:UNUSED_PAD src0_sel:DWORD src1_sel:WORD_1\n"
    "s_waitcnt lgkmcnt(3)\n"
    "ds_read_u16 v146, v146\n"
    "v_lshlrev_b32_sdwa v147, 1, v115 dst_sel:DWORD dst_unused:UNUSED_PAD src0_sel:DWORD src1_sel:WORD_1\n"
    "s_waitcnt lgkmcnt(3)\n"
    "ds_read_u16 v147, v147\n"
    "v_smfmac_f32_32x32x32_bf16 v[56:71], v[172:175], v[192:199], v12\n"
    "v_lshlrev_b32_sdwa v148, 1, v116 dst_sel:DWORD dst_unused:UNUSED_PAD src0_sel:DWORD src1_sel:WORD_1\n"
    "s_waitcnt lgkmcnt(3)\n"
    "ds_read_u16 v148, v148\n"
    "v_lshlrev_b32_sdwa v149, 1, v117 dst_sel:DWORD dst_unused:UNUSED_PAD src0_sel:DWORD src1_sel:WORD_1\n"
    "s_waitcnt lgkmcnt(3)\n"
    "ds_read_u16 v149, v149\n"
    "v_lshlrev_b32_sdwa v150, 1, v118 dst_sel:DWORD dst_unused:UNUSED_PAD src0_sel:DWORD src1_sel:WORD_1\n"
    "s_waitcnt lgkmcnt(3)\n"
    "ds_read_u16 v150, v150\n"
    "v_lshlrev_b32_sdwa v151, 1, v119 dst_sel:DWORD dst_unused:UNUSED_PAD src0_sel:DWORD src1_sel:WORD_1\n"
    "s_waitcnt lgkmcnt(3)\n"
    "ds_read_u16 v151, v151\n"
    "s_waitcnt vmcnt(6)\n"
    "ds_read_b64_tr_b16 v[200:201], v9 offset:2048\n"
    "ds_read_b64_tr_b16 v[202:203], v9 offset:2304\n"
    "ds_read_b64_tr_b16 v[204:205], v9 offset:3072\n"
    "ds_read_b64_tr_b16 v[206:207], v9 offset:3328\n"
    "v_mfma_f32_32x32x16_bf16 v[104:119], v[152:155], v[24:27], v[88:103]\n"
    "v_lshlrev_b32_sdwa v184, 1, v120 dst_sel:DWORD dst_unused:UNUSED_PAD src0_sel:DWORD src1_sel:WORD_1\n"
    "s_waitcnt lgkmcnt(3)\n"
    "ds_read_u16 v184, v184\n"
    "v_lshlrev_b32_sdwa v185, 1, v121 dst_sel:DWORD dst_unused:UNUSED_PAD src0_sel:DWORD src1_sel:WORD_1\n"
    "s_waitcnt lgkmcnt(3)\n"
    "ds_read_u16 v185, v185\n"
    "v_lshlrev_b32_sdwa v186, 1, v122 dst_sel:DWORD dst_unused:UNUSED_PAD src0_sel:DWORD src1_sel:WORD_1\n"
    "s_waitcnt lgkmcnt(3)\n"
    "ds_read_u16 v186, v186\n"
    "v_lshlrev_b32_sdwa v187, 1, v123 dst_sel:DWORD dst_unused:UNUSED_PAD src0_sel:DWORD src1_sel:WORD_1\n"
    "s_waitcnt lgkmcnt(3)\n"
    "ds_read_u16 v187, v187\n"
    "v_smfmac_f32_32x32x32_bf16 v[72:87], v[168:171], v[136:143], v12\n"
    "v_lshlrev_b32_sdwa v188, 1, v124 dst_sel:DWORD dst_unused:UNUSED_PAD src0_sel:DWORD src1_sel:WORD_1\n"
    "s_waitcnt lgkmcnt(3)\n"
    "ds_read_u16 v188, v188\n"
    "v_add_u32 v11, s85, v10\n"
    "v_lshlrev_b32_sdwa v189, 1, v125 dst_sel:DWORD dst_unused:UNUSED_PAD src0_sel:DWORD src1_sel:WORD_1\n"
    "s_waitcnt lgkmcnt(3)\n"
    "ds_read_u16 v189, v189\n"
    "v_lshlrev_b32_sdwa v190, 1, v126 dst_sel:DWORD dst_unused:UNUSED_PAD src0_sel:DWORD src1_sel:WORD_1\n"
    "s_waitcnt lgkmcnt(3)\n"
    "ds_read_u16 v190, v190\n"
    "v_lshlrev_b32_sdwa v191, 1, v127 dst_sel:DWORD dst_unused:UNUSED_PAD src0_sel:DWORD src1_sel:WORD_1\n"
    "s_waitcnt lgkmcnt(3)\n"
    "ds_read_u16 v191, v191\n"
    "v_mfma_f32_32x32x16_bf16 v[104:119], v[156:159], v[28:31], v[104:119]\n"
    "v_lshlrev_b32_sdwa v192, 1, v128 dst_sel:DWORD dst_unused:UNUSED_PAD src0_sel:DWORD src1_sel:WORD_1\n"
    "s_waitcnt lgkmcnt(3)\n"
    "ds_read_u16 v192, v192\n"
    "v_lshlrev_b32_sdwa v193, 1, v129 dst_sel:DWORD dst_unused:UNUSED_PAD src0_sel:DWORD src1_sel:WORD_1\n"
    "s_waitcnt lgkmcnt(3)\n"
    "ds_read_u16 v193, v193\n"
    "v_lshlrev_b32_sdwa v194, 1, v130 dst_sel:DWORD dst_unused:UNUSED_PAD src0_sel:DWORD src1_sel:WORD_1\n"
    "s_waitcnt lgkmcnt(3)\n"
    "ds_read_u16 v194, v194\n"
    "v_lshlrev_b32_sdwa v195, 1, v131 dst_sel:DWORD dst_unused:UNUSED_PAD src0_sel:DWORD src1_sel:WORD_1\n"
    "s_waitcnt lgkmcnt(3)\n"
    "ds_read_u16 v195, v195\n"
    "v_smfmac_f32_32x32x32_bf16 v[72:87], v[172:175], v[144:151], v12\n"
    "v_lshlrev_b32_sdwa v196, 1, v132 dst_sel:DWORD dst_unused:UNUSED_PAD src0_sel:DWORD src1_sel:WORD_1\n"
    "s_waitcnt lgkmcnt(3)\n"
    "ds_read_u16 v196, v196\n"
    "v_lshlrev_b32_sdwa v197, 1, v133 dst_sel:DWORD dst_unused:UNUSED_PAD src0_sel:DWORD src1_sel:WORD_1\n"
    "s_waitcnt lgkmcnt(3)\n"
    "ds_read_u16 v197, v197\n"
    "v_lshlrev_b32_sdwa v198, 1, v134 dst_sel:DWORD dst_unused:UNUSED_PAD src0_sel:DWORD src1_sel:WORD_1\n"
    "s_waitcnt lgkmcnt(3)\n"
    "ds_read_u16 v198, v198\n"
    "v_lshlrev_b32_sdwa v199, 1, v135 dst_sel:DWORD dst_unused:UNUSED_PAD src0_sel:DWORD src1_sel:WORD_1\n"
    "s_waitcnt lgkmcnt(3)\n"
    "ds_read_u16 v199, v199\n"
    "v_mfma_f32_32x32x16_bf16 v[120:135], v[152:155], v[32:35], v[88:103]\n"
    "v_lshlrev_b32_sdwa v136, 1, v104 dst_sel:DWORD dst_unused:UNUSED_PAD src0_sel:DWORD src1_sel:WORD_1\n"
    "s_waitcnt lgkmcnt(3)\n"
    "ds_read_u16 v136, v136\n"
    "s_add_u32 m0, s81, 6144\n"
    "s_nop 0\n"
    "buffer_load_dwordx4 v4, s[44:47], s79 offen lds\n"
    "v_lshlrev_b32_sdwa v137, 1, v105 dst_sel:DWORD dst_unused:UNUSED_PAD src0_sel:DWORD src1_sel:WORD_1\n"
    "s_waitcnt lgkmcnt(3)\n"
    "ds_read_u16 v137, v137\n"
    "v_lshlrev_b32_sdwa v138, 1, v106 dst_sel:DWORD dst_unused:UNUSED_PAD src0_sel:DWORD src1_sel:WORD_1\n"
    "s_waitcnt lgkmcnt(3)\n"
    "ds_read_u16 v138, v138\n"
    "v_lshlrev_b32_sdwa v139, 1, v107 dst_sel:DWORD dst_unused:UNUSED_PAD src0_sel:DWORD src1_sel:WORD_1\n"
    "s_waitcnt lgkmcnt(3)\n"
    "ds_read_u16 v139, v139\n"
    "v_smfmac_f32_32x32x32_bf16 v[40:55], v[200:203], v[184:191], v12\n"
    "v_lshlrev_b32_sdwa v140, 1, v108 dst_sel:DWORD dst_unused:UNUSED_PAD src0_sel:DWORD src1_sel:WORD_1\n"
    "s_waitcnt lgkmcnt(3)\n"
    "ds_read_u16 v140, v140\n"
    "buffer_load_dwordx4 v4, s[44:47], s79 offen offset:1024 lds\n"
    "v_lshlrev_b32_sdwa v141, 1, v109 dst_sel:DWORD dst_unused:UNUSED_PAD src0_sel:DWORD src1_sel:WORD_1\n"
    "s_waitcnt lgkmcnt(3)\n"
    "ds_read_u16 v141, v141\n"
    "v_lshlrev_b32_sdwa v142, 1, v110 dst_sel:DWORD dst_unused:UNUSED_PAD src0_sel:DWORD src1_sel:WORD_1\n"
    "s_waitcnt lgkmcnt(3)\n"
    "ds_read_u16 v142, v142\n"
    "v_lshlrev_b32_sdwa v143, 1, v111 dst_sel:DWORD dst_unused:UNUSED_PAD src0_sel:DWORD src1_sel:WORD_1\n"
    "s_waitcnt lgkmcnt(3)\n"
    "ds_read_u16 v143, v143\n"
    "v_mfma_f32_32x32x16_bf16 v[120:135], v[156:159], v[36:39], v[120:135]\n"
    "v_lshlrev_b32_sdwa v144, 1, v112 dst_sel:DWORD dst_unused:UNUSED_PAD src0_sel:DWORD src1_sel:WORD_1\n"
    "s_waitcnt lgkmcnt(3)\n"
    "ds_read_u16 v144, v144\n"
    "v_lshlrev_b32_sdwa v145, 1, v113 dst_sel:DWORD dst_unused:UNUSED_PAD src0_sel:DWORD src1_sel:WORD_1\n"
    "s_waitcnt lgkmcnt(3)\n"
    "ds_read_u16 v145, v145\n"
    "v_lshlrev_b32_sdwa v146, 1, v114 dst_sel:DWORD dst_unused:UNUSED_PAD src0_sel:DWORD src1_sel:WORD_1\n"
    "s_waitcnt lgkmcnt(3)\n"
    "ds_read_u16 v146, v146\n"
    "v_lshlrev_b32_sdwa v147, 1, v115 dst_sel:DWORD dst_unused:UNUSED_PAD src0_sel:DWORD src1_sel:WORD_1\n"
    "s_waitcnt lgkmcnt(3)\n"
    "ds_read_u16 v147, v147\n"
    "v_smfmac_f32_32x32x32_bf16 v[40:55], v[204:207], v[192:199], v12\n"
    "v_lshlrev_b32_sdwa v148, 1, v116 dst_sel:DWORD dst_unused:UNUSED_PAD src0_sel:DWORD src1_sel:WORD_1\n"
    "s_waitcnt lgkmcnt(3)\n"
    "ds_read_u16 v148, v148\n"
    "v_lshlrev_b32_sdwa v149, 1, v117 dst_sel:DWORD dst_unused:UNUSED_PAD src0_sel:DWORD src1_sel:WORD_1\n"
    "s_waitcnt lgkmcnt(3)\n"
    "ds_read_u16 v149, v149\n"
    "v_lshlrev_b32_sdwa v150, 1, v118 dst_sel:DWORD dst_unused:UNUSED_PAD src0_sel:DWORD src1_sel:WORD_1\n"
    "s_waitcnt lgkmcnt(3)\n"
    "ds_read_u16 v150, v150\n"
    "v_lshlrev_b32_sdwa v151, 1, v119 dst_sel:DWORD dst_unused:UNUSED_PAD src0_sel:DWORD src1_sel:WORD_1\n"
    "s_waitcnt lgkmcnt(3)\n"
    "ds_read_u16 v151, v151\n"
    "s_waitcnt vmcnt(2)\n"
    "ds_read_b64_tr_b16 v[160:161], v5 offset:0\n"
    "ds_read_b64_tr_b16 v[162:163], v6 offset:1024\n"
    "ds_read_b64_tr_b16 v[164:165], v5 offset:2048\n"
    "ds_read_b64_tr_b16 v[166:167], v6 offset:3072\n"
    "ds_read_u16 v176, v11 offset:0\n"
    "v_lshlrev_b32_sdwa v184, 1, v120 dst_sel:DWORD dst_unused:UNUSED_PAD src0_sel:DWORD src1_sel:WORD_1\n"
    "s_waitcnt lgkmcnt(3)\n"
    "ds_read_u16 v184, v184\n"
    "v_lshlrev_b32_sdwa v185, 1, v121 dst_sel:DWORD dst_unused:UNUSED_PAD src0_sel:DWORD src1_sel:WORD_1\n"
    "s_waitcnt lgkmcnt(3)\n"
    "ds_read_u16 v185, v185\n"
    "v_lshlrev_b32_sdwa v186, 1, v122 dst_sel:DWORD dst_unused:UNUSED_PAD src0_sel:DWORD src1_sel:WORD_1\n"
    "s_waitcnt lgkmcnt(3)\n"
    "ds_read_u16 v186, v186\n"
    "v_lshlrev_b32_sdwa v187, 1, v123 dst_sel:DWORD dst_unused:UNUSED_PAD src0_sel:DWORD src1_sel:WORD_1\n"
    "s_waitcnt lgkmcnt(3)\n"
    "ds_read_u16 v187, v187\n"
    "v_smfmac_f32_32x32x32_bf16 v[56:71], v[200:203], v[136:143], v12\n"
    "v_lshlrev_b32_sdwa v188, 1, v124 dst_sel:DWORD dst_unused:UNUSED_PAD src0_sel:DWORD src1_sel:WORD_1\n"
    "s_waitcnt lgkmcnt(3)\n"
    "ds_read_u16 v188, v188\n"
    "v_lshlrev_b32_sdwa v189, 1, v125 dst_sel:DWORD dst_unused:UNUSED_PAD src0_sel:DWORD src1_sel:WORD_1\n"
    "s_waitcnt lgkmcnt(3)\n"
    "ds_read_u16 v189, v189\n"
    "v_mfma_f32_32x32x16_bf16 v[88:103], v[176:179], v[180:183], 0\n"
    "v_lshlrev_b32_sdwa v190, 1, v126 dst_sel:DWORD dst_unused:UNUSED_PAD src0_sel:DWORD src1_sel:WORD_1\n"
    "s_waitcnt lgkmcnt(3)\n"
    "ds_read_u16 v190, v190\n"
    "v_lshlrev_b32_sdwa v191, 1, v127 dst_sel:DWORD dst_unused:UNUSED_PAD src0_sel:DWORD src1_sel:WORD_1\n"
    "s_waitcnt lgkmcnt(3)\n"
    "ds_read_u16 v191, v191\n"
    "v_lshlrev_b32_sdwa v192, 1, v128 dst_sel:DWORD dst_unused:UNUSED_PAD src0_sel:DWORD src1_sel:WORD_1\n"
    "s_waitcnt lgkmcnt(3)\n"
    "ds_read_u16 v192, v192\n"
    "v_mfma_f32_32x32x16_bf16 v[104:119], v[160:163], v[16:19], v[88:103]\n"
    "v_lshlrev_b32_sdwa v193, 1, v129 dst_sel:DWORD dst_unused:UNUSED_PAD src0_sel:DWORD src1_sel:WORD_1\n"
    "s_waitcnt lgkmcnt(3)\n"
    "ds_read_u16 v193, v193\n"
    "v_lshlrev_b32_sdwa v194, 1, v130 dst_sel:DWORD dst_unused:UNUSED_PAD src0_sel:DWORD src1_sel:WORD_1\n"
    "s_waitcnt lgkmcnt(3)\n"
    "ds_read_u16 v194, v194\n"
    "v_lshlrev_b32_sdwa v195, 1, v131 dst_sel:DWORD dst_unused:UNUSED_PAD src0_sel:DWORD src1_sel:WORD_1\n"
    "s_waitcnt lgkmcnt(3)\n"
    "ds_read_u16 v195, v195\n"
    "v_mfma_f32_32x32x16_bf16 v[104:119], v[164:167], v[20:23], v[104:119]\n"
    "v_smfmac_f32_32x32x32_bf16 v[56:71], v[204:207], v[144:151], v12\n"
    "v_lshlrev_b32_sdwa v196, 1, v132 dst_sel:DWORD dst_unused:UNUSED_PAD src0_sel:DWORD src1_sel:WORD_1\n"
    "s_waitcnt lgkmcnt(3)\n"
    "ds_read_u16 v196, v196\n"
    "v_lshlrev_b32_sdwa v197, 1, v133 dst_sel:DWORD dst_unused:UNUSED_PAD src0_sel:DWORD src1_sel:WORD_1\n"
    "s_waitcnt lgkmcnt(3)\n"
    "ds_read_u16 v197, v197\n"
    "v_lshlrev_b32_sdwa v198, 1, v134 dst_sel:DWORD dst_unused:UNUSED_PAD src0_sel:DWORD src1_sel:WORD_1\n"
    "s_waitcnt lgkmcnt(3)\n"
    "ds_read_u16 v198, v198\n"
    "v_lshlrev_b32_sdwa v199, 1, v135 dst_sel:DWORD dst_unused:UNUSED_PAD src0_sel:DWORD src1_sel:WORD_1\n"
    "s_waitcnt lgkmcnt(3)\n"
    "ds_read_u16 v199, v199\n"
    "s_sub_u32 s75, s75, 1\n"
    "s_cmp_lg_u32 s75, 0\n"
    "s_cbranch_scc1 .Lmx_q1\n"
    "v_smfmac_f32_32x32x32_bf16 v[72:87], v[200:203], v[184:191], v12\n"
    "s_waitcnt lgkmcnt(0)\n"
    "v_smfmac_f32_32x32x32_bf16 v[72:87], v[204:207], v[192:199], v12\n"
    "v_mov_b32 v184, 0\n"
    "v_mov_b32 v185, 0\n"
    "v_mov_b32 v186, 0\n"
    "v_mov_b32 v187, 0\n"
    "v_mov_b32 v188, 0\n"
    "v_mov_b32 v189, 0\n"
    "v_mov_b32 v190, 0\n"
    "v_mov_b32 v191, 0\n"
    "v_mov_b32 v192, 0\n"
    "v_mov_b32 v193, 0\n"
    "v_mov_b32 v194, 0\n"
    "v_mov_b32 v195, 0\n"
    "v_mov_b32 v196, 0\n"
    "v_mov_b32 v197, 0\n"
    "v_mov_b32 v198, 0\n"
    "v_mov_b32 v199, 0\n"
    "v_readfirstlane_b32 s80, v240\n"
    "s_add_u32 s80, s80, 1\n"
    "s_sub_u32 s86, s68, s69\n"
    "s_sub_u32 s86, s86, 2\n"
    "s_and_b32 s86, s86, 3\n"
    "s_mov_b32 s36, 1\n"
    "s_mov_b32 s38, 0\n"
    "s_mov_b32 s39, 0\n"
    "s_mov_b32 s37, 0\n"
    "s_cmp_eq_u32 s86, 3\n"
    "s_cbranch_scc1 .Lmx_tdl\n"
    "s_mov_b32 s36, 0\n"
    "s_lshl_b32 s86, s86, 12\n"
    "s_add_u32 s86, s86, 140288\n"
    "v_lshl_add_u32 v15, v1, 4, s86\n"
    "ds_write_b128 v15, v[72:75] offset:0\n"
    "ds_write_b128 v15, v[76:79] offset:1024\n"
    "ds_write_b128 v15, v[80:83] offset:2048\n"
    "ds_write_b128 v15, v[84:87] offset:3072\n"
    "v_mov_b32 v13, 162816\n"
    "v_mov_b32 v14, 1\n"
    "s_waitcnt lgkmcnt(0)\n"
    "s_mov_b64 exec, 1\n"
    "ds_add_u32 v13, v14\n"
    "s_mov_b64 exec, -1\n"
    ".Lmx_tdl:\n"
    ".p2align 6\n"
    ".Lmx_q2:\n"
    "ds_read_b64_tr_b16 v[168:169], v9 offset:0\n"
    "ds_read_b64_tr_b16 v[170:171], v9 offset:256\n"
    "ds_read_b64_tr_b16 v[172:173], v9 offset:1024\n"
    "ds_read_b64_tr_b16 v[174:175], v9 offset:1280\n"
    "v_mfma_f32_32x32x16_bf16 v[120:135], v[160:163], v[24:27], v[88:103]\n"
    "v_lshlrev_b32_sdwa v136, 1, v104 dst_sel:DWORD dst_unused:UNUSED_PAD src0_sel:DWORD src1_sel:WORD_1\n"
    "s_waitcnt lgkmcnt(3)\n"
    "ds_read_u16 v136, v136\n"
    "v_lshlrev_b32_sdwa v137, 1, v105 dst_sel:DWORD dst_unused:UNUSED_PAD src0_sel:DWORD src1_sel:WORD_1\n"
    "s_waitcnt lgkmcnt(3)\n"
    "ds_read_u16 v137, v137\n"
    "v_lshlrev_b32_sdwa v138, 1, v106 dst_sel:DWORD dst_unused:UNUSED_PAD src0_sel:DWORD src1_sel:WORD_1\n"
    "s_waitcnt lgkmcnt(3)\n"
    "ds_read_u16 v138, v138\n"
    "v_lshlrev_b32_sdwa v139, 1, v107 dst_sel:DWORD dst_unused:UNUSED_PAD src0_sel:DWORD src1_sel:WORD_1\n"
    "s_waitcnt lgkmcnt(3)\n"
    "ds_read_u16 v139, v139\n"
    "v_smfmac_f32_32x32x32_bf16 v[56:71], v[200:203], v[184:191], v12\n"
    "v_lshlrev_b32_sdwa v140, 1, v108 dst_sel:DWORD dst_unused:UNUSED_PAD src0_sel:DWORD src1_sel:WORD_1\n"
    "s_waitcnt lgkmcnt(3)\n"
    "ds_read_u16 v140, v140\n"
    "s_add_u32 s74, s74, 1\n"
    "s_cmp_eq_u32 s74, 11\n"
    "s_cselect_b32 s74, 0, s74\n"
    "s_add_u32 s86, s92, s74\n"
    "s_lshl_b32 s76, s86, 7\n"
    "v_lshlrev_b32_sdwa v141, 1, v109 dst_sel:DWORD dst_unused:UNUSED_PAD src0_sel:DWORD src1_sel:WORD_1\n"
    "s_waitcnt lgkmcnt(3)\n"
    "ds_read_u16 v141, v141\n"
    "ds_read_b64_tr_b16 v[152:153], v7 offset:0\n"
    "ds_read_b64_tr_b16 v[154:155], v8 offset:1024\n"
    "ds_read_b64_tr_b16 v[156:157], v7 offset:2048\n"
    "ds_read_b64_tr_b16 v[158:159], v8 offset:3072\n"
    "v_lshlrev_b32_sdwa v142, 1, v110 dst_sel:DWORD dst_unused:UNUSED_PAD src0_sel:DWORD src1_sel:WORD_1\n"
    "s_waitcnt lgkmcnt(3)\n"
    "ds_read_u16 v142, v142\n"
    "v_lshlrev_b32_sdwa v143, 1, v111 dst_sel:DWORD dst_unused:UNUSED_PAD src0_sel:DWORD src1_sel:WORD_1\n"
    "s_waitcnt lgkmcnt(3)\n"
    "ds_read_u16 v143, v143\n"
    "v_mfma_f32_32x32x16_bf16 v[120:135], v[164:167], v[28:31], v[120:135]\n"
    "v_lshlrev_b32_sdwa v144, 1, v112 dst_sel:DWORD dst_unused:UNUSED_PAD src0_sel:DWORD src1_sel:WORD_1\n"
    "s_waitcnt lgkmcnt(3)\n"
    "ds_read_u16 v144, v144\n"
    "s_add_u32 s77, s76, 145408\n"
    "s_lshl_b32 s78, s86, 12\n"
    "s_add_u32 s79, s78, 2048\n"
    "s_lshl_b32 s85, s74, 7\n"
    "v_lshlrev_b32_sdwa v145, 1, v113 dst_sel:DWORD dst_unused:UNUSED_PAD src0_sel:DWORD src1_sel:WORD_1\n"
    "s_waitcnt lgkmcnt(3)\n"
    "ds_read_u16 v145, v145\n"
    "ds_read_u16 v176, v11 offset:64\n"
    "v_lshlrev_b32_sdwa v146, 1, v114 dst_sel:DWORD dst_unused:UNUSED_PAD src0_sel:DWORD src1_sel:WORD_1\n"
    "s_waitcnt lgkmcnt(3)\n"
    "ds_read_u16 v146, v146\n"
    "v_lshlrev_b32_sdwa v147, 1, v115 dst_sel:DWORD dst_unused:UNUSED_PAD src0_sel:DWORD src1_sel:WORD_1\n"
    "s_waitcnt lgkmcnt(3)\n"
    "ds_read_u16 v147, v147\n"
    "v_smfmac_f32_32x32x32_bf16 v[56:71], v[204:207], v[192:199], v12\n"
    "v_lshlrev_b32_sdwa v148, 1, v116 dst_sel:DWORD dst_unused:UNUSED_PAD src0_sel:DWORD src1_sel:WORD_1\n"
    "s_waitcnt lgkmcnt(3)\n"
    "ds_read_u16 v148, v148\n"
    "v_lshlrev_b32_sdwa v149, 1, v117 dst_sel:DWORD dst_unused:UNUSED_PAD src0_sel:DWORD src1_sel:WORD_1\n"
    "s_waitcnt lgkmcnt(3)\n"
    "ds_read_u16 v149, v149\n"
    "v_lshlrev_b32_sdwa v150, 1, v118 dst_sel:DWORD dst_unused:UNUSED_PAD src0_sel:DWORD src1_sel:WORD_1\n"
    "s_waitcnt lgkmcnt(3)\n"
    "ds_read_u16 v150, v150\n"
    "v_lshlrev_b32_sdwa v151, 1, v119 dst_sel:DWORD dst_unused:UNUSED_PAD src0_sel:DWORD src1_sel:WORD_1\n"
    "s_waitcnt lgkmcnt(3)\n"
    "ds_read_u16 v151, v151\n"
    "v_lshlrev_b32_sdwa v184, 1, v120 dst_sel:DWORD dst_unused:UNUSED_PAD src0_sel:DWORD src1_sel:WORD_1\n"
    "s_waitcnt lgkmcnt(3)\n"
    "ds_read_u16 v184, v184\n"
    "s_add_u32 m0, s81, 4096\n"
    "s_nop 0\n"
    "buffer_load_dwordx4 v4, s[44:47], s78 offen lds\n"
    "v_lshlrev_b32_sdwa v185, 1, v121 dst_sel:DWORD dst_unused:UNUSED_PAD src0_sel:DWORD src1_sel:WORD_1\n"
    "s_waitcnt lgkmcnt(3)\n"
    "ds_read_u16 v185, v185\n"
    "v_mfma_f32_32x32x16_bf16 v[88:103], v[176:179], v[180:183], 0\n"
    "v_lshlrev_b32_sdwa v186, 1, v122 dst_sel:DWORD dst_unused:UNUSED_PAD src0_sel:DWORD src1_sel:WORD_1\n"
    "s_waitcnt lgkmcnt(3)\n"
    "ds_read_u16 v186, v186\n"
    "v_lshlrev_b32_sdwa v187, 1, v123 dst_sel:DWORD dst_unused:UNUSED_PAD src0_sel:DWORD src1_sel:WORD_1\n"
    "s_waitcnt lgkmcnt(3)\n"
    "ds_read_u16 v187, v187\n"
    "v_smfmac_f32_32x32x32_bf16 v[40:55], v[168:171], v[136:143], v12\n"
    "v_lshlrev_b32_sdwa v188, 1, v124 dst_sel:DWORD dst_unused:UNUSED_PAD src0_sel:DWORD src1_sel:WORD_1\n"
    "s_waitcnt lgkmcnt(3)\n"
    "ds_read_u16 v188, v188\n"
    "buffer_load_dwordx4 v4, s[44:47], s78 offen offset:1024 lds\n"
    "v_lshlrev_b32_sdwa v189, 1, v125 dst_sel:DWORD dst_unused:UNUSED_PAD src0_sel:DWORD src1_sel:WORD_1\n"
    "s_waitcnt lgkmcnt(3)\n"
    "ds_read_u16 v189, v189\n"
    "v_mfma_f32_32x32x16_bf16 v[104:119], v[152:155], v[16:19], v[88:103]\n"
    "v_lshlrev_b32_sdwa v190, 1, v126 dst_sel:DWORD dst_unused:UNUSED_PAD src0_sel:DWORD src1_sel:WORD_1\n"
    "s_waitcnt lgkmcnt(3)\n"
    "ds_read_u16 v190, v190\n"
    "v_lshlrev_b32_sdwa v191, 1, v127 dst_sel:DWORD dst_unused:UNUSED_PAD src0_sel:DWORD src1_sel:WORD_1\n"
    "s_waitcnt lgkmcnt(3)\n"
    "ds_read_u16 v191, v191\n"
    "v_lshlrev_b32_sdwa v192, 1, v128 dst_sel:DWORD dst_unused:UNUSED_PAD src0_sel:DWORD src1_sel:WORD_1\n"
    "s_waitcnt lgkmcnt(3)\n"
    "ds_read_u16 v192, v192\n"
    "s_mov_b32 m0, s81\n"
    "s_nop 0\n"
    "buffer_load_dwordx4 v2, s[40:43], s76 offen lds\n"
    "buffer_load_dwordx4 v3, s[40:43], s76 offen offset:1024 lds\n"
    "v_lshlrev_b32_sdwa v193, 1, v129 dst_sel:DWORD dst_unused:UNUSED_PAD src0_sel:DWORD src1_sel:WORD_1\n"
    "s_waitcnt lgkmcnt(3)\n"
    "ds_read_u16 v193, v193\n"
    "v_mfma_f32_32x32x16_bf16 v[104:119], v[156:159], v[20:23], v[104:119]\n"
    "v_lshlrev_b32_sdwa v194, 1, v130 dst_sel:DWORD dst_unused:UNUSED_PAD src0_sel:DWORD src1_sel:WORD_1\n"
    "s_waitcnt lgkmcnt(3)\n"
    "ds_read_u16 v194, v194\n"
    "v_lshlrev_b32_sdwa v195, 1, v131 dst_sel:DWORD dst_unused:UNUSED_PAD src0_sel:DWORD src1_sel:WORD_1\n"
    "s_waitcnt lgkmcnt(3)\n"
    "ds_read_u16 v195, v195\n"
    "v_smfmac_f32_32x32x32_bf16 v[40:55], v[172:175], v[144:151], v12\n"
    "v_lshlrev_b32_sdwa v196, 1, v132 dst_sel:DWORD dst_unused:UNUSED_PAD src0_sel:DWORD src1_sel:WORD_1\n"
    "s_waitcnt lgkmcnt(3)\n"
    "ds_read_u16 v196, v196\n"
    "buffer_load_dwordx4 v2, s[40:43], s77 offen offset:2048 lds\n"
    "buffer_load_dwordx4 v3, s[40:43], s77 offen offset:3072 lds\n"
    "v_lshlrev_b32_sdwa v197, 1, v133 dst_sel:DWORD dst_unused:UNUSED_PAD src0_sel:DWORD src1_sel:WORD_1\n"
    "s_waitcnt lgkmcnt(3)\n"
    "ds_read_u16 v197, v197\n"
    "v_lshlrev_b32_sdwa v198, 1, v134 dst_sel:DWORD dst_unused:UNUSED_PAD src0_sel:DWORD src1_sel:WORD_1\n"
    "s_waitcnt lgkmcnt(3)\n"
    "ds_read_u16 v198, v198\n"
    "v_lshlrev_b32_sdwa v199, 1, v135 dst_sel:DWORD dst_unused:UNUSED_PAD src0_sel:DWORD src1_sel:WORD_1\n"
    "s_waitcnt lgkmcnt(3)\n"
    "ds_read_u16 v199, v199\n"
    "s_waitcnt vmcnt(6)\n"
    "ds_read_b64_tr_b16 v[200:201], v9 offset:2048\n"
    "ds_read_b64_tr_b16 v[202:203], v9 offset:2304\n"
    "ds_read_b64_tr_b16 v[204:205], v9 offset:3072\n"
    "ds_read_b64_tr_b16 v[206:207], v9 offset:3328\n"
    "v_mfma_f32_32x32x16_bf16 v[120:135], v[152:155], v[24:27], v[88:103]\n"
    "v_lshlrev_b32_sdwa v136, 1, v104 dst_sel:DWORD dst_unused:UNUSED_PAD src0_sel:DWORD src1_sel:WORD_1\n"
    "s_waitcnt lgkmcnt(3)\n"
    "ds_read_u16 v136, v136\n"
    "v_lshlrev_b32_sdwa v137, 1, v105 dst_sel:DWORD dst_unused:UNUSED_PAD src0_sel:DWORD src1_sel:WORD_1\n"
    "s_waitcnt lgkmcnt(3)\n"
    "ds_read_u16 v137, v137\n"
    "v_lshlrev_b32_sdwa v138, 1, v106 dst_sel:DWORD dst_unused:UNUSED_PAD src0_sel:DWORD src1_sel:WORD_1\n"
    "s_waitcnt lgkmcnt(3)\n"
    "ds_read_u16 v138, v138\n"
    "v_lshlrev_b32_sdwa v139, 1, v107 dst_sel:DWORD dst_unused:UNUSED_PAD src0_sel:DWORD src1_sel:WORD_1\n"
    "s_waitcnt lgkmcnt(3)\n"
    "ds_read_u16 v139, v139\n"
    "v_smfmac_f32_32x32x32_bf16 v[56:71], v[168:171], v[184:191], v12\n"
    "v_lshlrev_b32_sdwa v140, 1, v108 dst_sel:DWORD dst_unused:UNUSED_PAD src0_sel:DWORD src1_sel:WORD_1\n"
    "s_waitcnt lgkmcnt(3)\n"
    "ds_read_u16 v140, v140\n"
    "v_add_u32 v11, s85, v10\n"
    "v_lshlrev_b32_sdwa v141, 1, v109 dst_sel:DWORD dst_unused:UNUSED_PAD src0_sel:DWORD src1_sel:WORD_1\n"
    "s_waitcnt lgkmcnt(3)\n"
    "ds_read_u16 v141, v141\n"
    "v_lshlrev_b32_sdwa v142, 1, v110 dst_sel:DWORD dst_unused:UNUSED_PAD src0_sel:DWORD src1_sel:WORD_1\n"
    "s_waitcnt lgkmcnt(3)\n"
    "ds_read_u16 v142, v142\n"
    "v_lshlrev_b32_sdwa v143, 1, v111 dst_sel:DWORD dst_unused:UNUSED_PAD src0_sel:DWORD src1_sel:WORD_1\n"
    "s_waitcnt lgkmcnt(3)\n"
    "ds_read_u16 v143, v143\n"
    "v_mfma_f32_32x32x16_bf16 v[120:135], v[156:159], v[28:31], v[120:135]\n"
    "v_lshlrev_b32_sdwa v144, 1, v112 dst_sel:DWORD dst_unused:UNUSED_PAD src0_sel:DWORD src1_sel:WORD_1\n"
    "s_waitcnt lgkmcnt(3)\n"
    "ds_read_u16 v144, v144\n"
    "v_lshlrev_b32_sdwa v145, 1, v113 dst_sel:DWORD dst_unused:UNUSED_PAD src0_sel:DWORD src1_sel:WORD_1\n"
    "s_waitcnt lgkmcnt(3)\n"
    "ds_read_u16 v145, v145\n"
    "v_lshlrev_b32_sdwa v146, 1, v114 dst_sel:DWORD dst_unused:UNUSED_PAD src0_sel:DWORD src1_sel:WORD_1\n"
    "s_waitcnt lgkmcnt(3)\n"
    "ds_read_u16 v146, v146\n"
    "v_lshlrev_b32_sdwa v147, 1, v115 dst_sel:DWORD dst_unused:UNUSED_PAD src0_sel:DWORD src1_sel:WORD_1\n"
    "s_waitcnt lgkmcnt(3)\n"
    "ds_read_u16 v147, v147\n"
    "v_smfmac_f32_32x32x32_bf16 v[56:71], v[172:175], v[192:199], v12\n"
    "v_lshlrev_b32_sdwa v148, 1, v116 dst_sel:DWORD dst_unused:UNUSED_PAD src0_sel:DWORD src1_sel:WORD_1\n"
    "s_waitcnt lgkmcnt(3)\n"
    "ds_read_u16 v148, v148\n"
    "v_lshlrev_b32_sdwa v149, 1, v117 dst_sel:DWORD dst_unused:UNUSED_PAD src0_sel:DWORD src1_sel:WORD_1\n"
    "s_waitcnt lgkmcnt(3)\n"
    "ds_read_u16 v149, v149\n"
    "v_lshlrev_b32_sdwa v150, 1, v118 dst_sel:DWORD dst_unused:UNUSED_PAD src0_sel:DWORD src1_sel:WORD_1\n"
    "s_waitcnt lgkmcnt(3)\n"
    "ds_read_u16 v150, v150\n"
    "v_lshlrev_b32_sdwa v151, 1, v119 dst_sel:DWORD dst_unused:UNUSED_PAD src0_sel:DWORD src1_sel:WORD_1\n"
    "s_waitcnt lgkmcnt(3)\n"
    "ds_read_u16 v151, v151\n"
    "s_waitcnt vmcnt(0)\n"
    "ds_read_b64_tr_b16 v[160:161], v5 offset:0\n"
    "ds_read_b64_tr_b16 v[162:163], v6 offset:1024\n"
    "ds_read_b64_tr_b16 v[164:165], v5 offset:2048\n"
    "ds_read_b64_tr_b16 v[166:167], v6 offset:3072\n"
    "ds_read_u16 v176, v11 offset:0\n"
    "v_lshlrev_b32_sdwa v184, 1, v120 dst_sel:DWORD dst_unused:UNUSED_PAD src0_sel:DWORD src1_sel:WORD_1\n"
    "s_waitcnt lgkmcnt(3)\n"
    "ds_read_u16 v184, v184\n"
    "s_add_u32 m0, s81, 6144\n"
    "s_nop 0\n"
    "buffer_load_dwordx4 v4, s[44:47], s79 offen lds\n"
    "v_lshlrev_b32_sdwa v185, 1, v121 dst_sel:DWORD dst_unused:UNUSED_PAD src0_sel:DWORD src1_sel:WORD_1\n"
    "s_waitcnt lgkmcnt(3)\n"
    "ds_read_u16 v185, v185\n"
    "v_lshlrev_b32_sdwa v186, 1, v122 dst_sel:DWORD dst_unused:UNUSED_PAD src0_sel:DWORD src1_sel:WORD_1\n"
    "s_waitcnt lgkmcnt(3)\n"
    "ds_read_u16 v186, v186\n"
    "v_lshlrev_b32_sdwa v187, 1, v123 dst_sel:DWORD dst_unused:UNUSED_PAD src0_sel:DWORD src1_sel:WORD_1\n"
    "s_waitcnt lgkmcnt(3)\n"
    "ds_read_u16 v187, v187\n"
    "v_smfmac_f32_32x32x32_bf16 v[40:55], v[200:203], v[136:143], v12\n"
    "v_lshlrev_b32_sdwa v188, 1, v124 dst_sel:DWORD dst_unused:UNUSED_PAD src0_sel:DWORD src1_sel:WORD_1\n"
    "s_waitcnt lgkmcnt(3)\n"
    "ds_read_u16 v188, v188\n"
    "buffer_load_dwordx4 v4, s[44:47], s79 offen offset:1024 lds\n"
    "v_lshlrev_b32_sdwa v189, 1, v125 dst_sel:DWORD dst_unused:UNUSED_PAD src0_sel:DWORD src1_sel:WORD_1\n"
    "s_waitcnt lgkmcnt(3)\n"
    "ds_read_u16 v189, v189\n"
    "v_mfma_f32_32x32x16_bf16 v[88:103], v[176:179], v[180:183], 0\n"
    "v_lshlrev_b32_sdwa v190, 1, v126 dst_sel:DWORD dst_unused:UNUSED_PAD src0_sel:DWORD src1_sel:WORD_1\n"
    "s_waitcnt lgkmcnt(3)\n"
    "ds_read_u16 v190, v190\n"
    "v_lshlrev_b32_sdwa v191, 1, v127 dst_sel:DWORD dst_unused:UNUSED_PAD src0_sel:DWORD src1_sel:WORD_1\n"
    "s_waitcnt lgkmcnt(3)\n"
    "ds_read_u16 v191, v191\n"
    "v_lshlrev_b32_sdwa v192, 1, v128 dst_sel:DWORD dst_unused:UNUSED_PAD src0_sel:DWORD src1_sel:WORD_1\n"
    "s_waitcnt lgkmcnt(3)\n"
    "ds_read_u16 v192, v192\n"
    "v_mfma_f32_32x32x16_bf16 v[104:119], v[160:163], v[16:19], v[88:103]\n"
    "v_lshlrev_b32_sdwa v193, 1, v129 dst_sel:DWORD dst_unused:UNUSED_PAD src0_sel:DWORD src1_sel:WORD_1\n"
    "s_waitcnt lgkmcnt(3)\n"
    "ds_read_u16 v193, v193\n"
    "v_lshlrev_b32_sdwa v194, 1, v130 dst_sel:DWORD dst_unused:UNUSED_PAD src0_sel:DWORD src1_sel:WORD_1\n"
    "s_waitcnt lgkmcnt(3)\n"
    "ds_read_u16 v194, v194\n"
    "v_lshlrev_b32_sdwa v195, 1, v131 dst_sel:DWORD dst_unused:UNUSED_PAD src0_sel:DWORD src1_sel:WORD_1\n"
    "s_waitcnt lgkmcnt(3)\n"
    "ds_read_u16 v195, v195\n"
    "v_mfma_f32_32x32x16_bf16 v[104:119], v[164:167], v[20:23], v[104:119]\n"
    "v_smfmac_f32_32x32x32_bf16 v[40:55], v[204:207], v[144:151], v12\n"
    "v_lshlrev_b32_sdwa v196, 1, v132 dst_sel:DWORD dst_unused:UNUSED_PAD src0_sel:DWORD src1_sel:WORD_1\n"
    "s_waitcnt lgkmcnt(3)\n"
    "ds_read_u16 v196, v196\n"
    "v_lshlrev_b32_sdwa v197, 1, v133 dst_sel:DWORD dst_unused:UNUSED_PAD src0_sel:DWORD src1_sel:WORD_1\n"
    "s_waitcnt lgkmcnt(3)\n"
    "ds_read_u16 v197, v197\n"
    "v_lshlrev_b32_sdwa v198, 1, v134 dst_sel:DWORD dst_unused:UNUSED_PAD src0_sel:DWORD src1_sel:WORD_1\n"
    "s_waitcnt lgkmcnt(3)\n"
    "ds_read_u16 v198, v198\n"
    "v_lshlrev_b32_sdwa v199, 1, v135 dst_sel:DWORD dst_unused:UNUSED_PAD src0_sel:DWORD src1_sel:WORD_1\n"
    "s_waitcnt lgkmcnt(3)\n"
    "ds_read_u16 v199, v199\n"
    ".Lmx_pfl:\n"
    "s_cmp_eq_u32 s93, 1\n"
    "s_cbranch_scc0 .Lmx_npl\n"
    "s_lshl_b32 s88, s73, 11\n"
    "s_lshl_b32 s86, s69, 8\n"
    "s_add_u32 s88, s88, s86\n"
    "s_add_u32 s88, s88, 1048576\n"
    "s_add_u32 s88, s88, s37\n"
    "v_mov_b32 v13, s80\n"
    "s_mov_b64 exec, 1\n"
    "buffer_store_dword v13, off, s[56:59], s88 sc1\n"
    "s_mov_b64 exec, -1\n"
    "s_mov_b32 s93, 0\n"
    ".Lmx_npl:\n"
    "s_cmp_lg_u32 s36, 0\n"
    "s_cbranch_scc1 .Lmx_bl\n"
    ".Lmx_rl:\n"
    "s_sub_u32 s84, s84, 1\n"
    "s_cmp_lg_u32 s84, 0\n"
    "s_cbranch_scc1 .Lmx_q2\n"
    "s_cmp_eq_u32 s36, 1\n"
    "s_cselect_b32 s84, 1, s93\n"
    "s_cmp_eq_u32 s84, 0\n"
    "s_cbranch_scc1 .Lmx_okl\n"
    "s_add_u32 s39, s39, 1\n"
    "s_cmp_lt_u32 s39, 0x400000\n"
    "s_cbranch_scc0 .Lmx_okl\n"
    "s_waitcnt vmcnt(0)\n"
    "s_branch .Lmx_pfl\n"
    ".Lmx_okl:\n"
    "v_smfmac_f32_32x32x32_bf16 v[56:71], v[200:203], v[184:191], v12\n"
    "s_waitcnt lgkmcnt(0)\n"
    "v_smfmac_f32_32x32x32_bf16 v[56:71], v[204:207], v[192:199], v12\n"
    "s_setprio 0\n"
    ".Lmx_epi:\n"
    "s_cmp_lg_u32 s68, s94\n"
    "s_cbranch_scc1 .Lmx_nofin1\n"
    "s_cmp_eq_u32 s38, 1\n"
    "s_cbranch_scc1 .Lmx_nofin1\n"
    "s_waitcnt vmcnt(0)\n"
    "buffer_load_dword v243, v242, s[56:59], 0 offen sc1\n"
    ".Lmx_nofin1:\n"
    "v_lshlrev_b32 v142, 4, v1\n"
    "v_add_u32 v136, s81, v142\n"
    "s_and_b32 s87, s68, 3\n"
    "ds_write_b128 v136, v[40:43] offset:0\n"
    "ds_write_b128 v136, v[44:47] offset:1024\n"
    "ds_write_b128 v136, v[48:51] offset:2048\n"
    "ds_write_b128 v136, v[52:55] offset:3072\n"
    "s_waitcnt vmcnt(0)\n"
    "s_nop 0\n"
    "ds_write_b128 v136, v[56:59] offset:4096\n"
    "ds_write_b128 v136, v[60:63] offset:5120\n"
    "ds_write_b128 v136, v[64:67] offset:6144\n"
    "ds_write_b128 v136, v[68:71] offset:7168\n"
    "s_cmp_lg_u32 s68, s94\n"
    "s_cbranch_scc1 .Lmx_nofin2\n"
    "s_cmp_eq_u32 s38, 1\n"
    "s_cbranch_scc1 .Lmx_fb\n"
    "s_mov_b32 s87, 0\n"
    "s_branch .Lmx_chk\n"
    ".Lmx_poll:\n"
    "s_sleep 1\n"
    "buffer_load_dword v243, v242, s[56:59], 0 offen sc1\n"
    ".Lmx_chk:\n"
    "s_waitcnt vmcnt(0)\n"
    "v_cmp_ne_u32 vcc, s80, v243\n"
    "s_nop 4\n"
    "s_cbranch_vccz .Lmx_go\n"
    "s_add_u32 s87, s87, 1\n"
    "s_cmp_lt_u32 s87, 0x40000\n"
    "s_cbranch_scc1 .Lmx_poll\n"
    ".Lmx_go:\n"
    "buffer_load_dwordx4 v[216:219], v244, s[56:59], 0 offen sc1\n"
    "s_mov_b32 s86, 4096\n"
    "buffer_load_dwordx4 v[220:223], v244, s[56:59], s86 offen sc1\n"
    "s_mov_b32 s86, 8192\n"
    "buffer_load_dwordx4 v[224:227], v244, s[56:59], s86 offen sc1\n"
    "s_mov_b32 s86, 12288\n"
    "buffer_load_dwordx4 v[228:231], v244, s[56:59], s86 offen sc1\n"
    "s_mov_b32 s86, 2097152\n"
    "buffer_load_dwordx4 v[72:75], v244, s[56:59], s86 offen sc1\n"
    "s_mov_b32 s86, 2101248\n"
    "buffer_load_dwordx4 v[76:79], v244, s[56:59], s86 offen sc1\n"
    "s_mov_b32 s86, 2105344\n"
    "buffer_load_dwordx4 v[80:83], v244, s[56:59], s86 offen sc1\n"
    "s_mov_b32 s86, 2109440\n"
    "buffer_load_dwordx4 v[84:87], v244, s[56:59], s86 offen sc1\n"
    "s_cmp_lg_u32 s69, 0\n"
    "s_cbranch_scc1 .Lmx_nepb\n"
    "s_lshl_b32 s86, s73, 11\n"
    "s_add_u32 s86, s86, 1049600\n"
    "v_mov_b32 v13, s80\n"
    "s_mov_b64 exec, 1\n"
    "buffer_store_dword v13, off, s[56:59], s86 sc1\n"
    "s_mov_b64 exec, -1\n"
    ".Lmx_nepb:\n"
    "s_and_b32 s87, s68, 3\n"
    ".Lmx_fb:\n"
    "s_mov_b32 s89, 0x3f888e99\n"
    "v_lshlrev_b32 v144, 16, v234\n"
    "v_and_b32 v145, 0xffff0000, v234\n"
    "v_lshlrev_b32 v146, 16, v235\n"
    "v_and_b32 v147, 0xffff0000, v235\n"
    "s_waitcnt vmcnt(3)\n"
    "v_pk_add_f32 v[216:217], v[216:217], v[72:73]\n"
    "v_pk_add_f32 v[218:219], v[218:219], v[74:75]\n"
    "s_waitcnt vmcnt(2)\n"
    "v_pk_add_f32 v[220:221], v[220:221], v[76:77]\n"
    "v_pk_add_f32 v[222:223], v[222:223], v[78:79]\n"
    "s_waitcnt vmcnt(1)\n"
    "v_pk_add_f32 v[224:225], v[224:225], v[80:81]\n"
    "v_pk_add_f32 v[226:227], v[226:227], v[82:83]\n"
    "s_waitcnt vmcnt(0)\n"
    "v_pk_add_f32 v[228:229], v[228:229], v[84:85]\n"
    "v_pk_add_f32 v[230:231], v[230:231], v[86:87]\n"
    "v_pk_add_f32 v[216:217], v[216:217], v[220:221]\n"
    "v_pk_add_f32 v[224:225], v[224:225], v[228:229]\n"
    "v_pk_add_f32 v[218:219], v[218:219], v[222:223]\n"
    "v_pk_add_f32 v[226:227], v[226:227], v[230:231]\n"
    "v_pk_add_f32 v[216:217], v[216:217], v[224:225]\n"
    "v_pk_add_f32 v[218:219], v[218:219], v[226:227]\n"
    "v_fma_f32 v216, v216, s89, v144\n"
    "v_fma_f32 v217, v217, s89, v145\n"
    "v_fma_f32 v218, v218, s89, v146\n"
    "v_fma_f32 v219, v219, s89, v147\n"
    "v_cvt_pk_bf16_f32 v216, v216, v217\n"
    "v_cvt_pk_bf16_f32 v217, v218, v219\n"
    "v_lshrrev_b32 v139, 3, v1\n"
    "v_mul_u32_u24 v139, 4608, v139\n"
    "v_and_b32 v148, 7, v1\n"
    "v_lshl_add_u32 v139, v148, 1, v139\n"
    "s_lshl_b32 s86, s69, 4\n"
    "s_add_u32 s88, s91, s86\n"
    "buffer_store_short v216, v139, s[52:55], s88 offen sc1\n"
    "buffer_store_short_d16_hi v216, v139, s[52:55], s88 offen offset:1152 sc1\n"
    "buffer_store_short v217, v139, s[52:55], s88 offen offset:2304 sc1\n"
    "buffer_store_short_d16_hi v217, v139, s[52:55], s88 offen offset:3456 sc1\n"
    ".Lmx_nofin2:\n"
    "s_lshr_b32 s86, s68, 2\n"
    "s_lshl_b32 s88, s86, 12\n"
    "s_lshl_b32 s89, s87, 10\n"
    "s_add_u32 s88, s88, s89\n"
    "s_add_u32 s88, s88, 32768\n"
    "v_add_u32 v138, s88, v142\n"
    "v_add_u32 v143, 65536, v138\n"
    "v_lshlrev_b32 v144, 16, v232\n"
    "v_and_b32 v145, 0xffff0000, v232\n"
    "v_lshlrev_b32 v146, 16, v233\n"
    "v_and_b32 v147, 0xffff0000, v233\n"
    "s_mov_b32 s89, 0x3f888e99\n"
    "v_lshrrev_b32 v139, 5, v1\n"
    "v_mul_u32_u24 v139, 4608, v139\n"
    "v_and_b32 v148, 31, v1\n"
    "v_lshl_add_u32 v139, v148, 1, v139\n"
    "s_mul_i32 s88, s87, 9216\n"
    "s_lshr_b32 s86, s68, 2\n"
    "s_lshl_b32 s86, s86, 6\n"
    "s_add_u32 s88, s88, s86\n"
    "s_add_u32 s88, s88, s72\n"
    "s_waitcnt lgkmcnt(0)\n"
    "s_barrier\n"
    "ds_read_b128 v[104:107], v138 offset:0\n"
    "ds_read_b128 v[108:111], v138 offset:8192\n"
    "ds_read_b128 v[112:115], v138 offset:16384\n"
    "ds_read_b128 v[116:119], v138 offset:24576\n"
    "ds_read_b128 v[120:123], v143 offset:0\n"
    "ds_read_b128 v[124:127], v143 offset:8192\n"
    "ds_read_b128 v[128:131], v143 offset:16384\n"
    "ds_read_b128 v[132:135], v143 offset:24576\n"
    "s_waitcnt lgkmcnt(6)\n"
    "v_pk_add_f32 v[104:105], v[104:105], v[108:109]\n"
    "v_pk_add_f32 v[106:107], v[106:107], v[110:111]\n"
    "s_waitcnt lgkmcnt(4)\n"
    "v_pk_add_f32 v[112:113], v[112:113], v[116:117]\n"
    "v_pk_add_f32 v[114:115], v[114:115], v[118:119]\n"
    "s_waitcnt lgkmcnt(2)\n"
    "v_pk_add_f32 v[120:121], v[120:121], v[124:125]\n"
    "v_pk_add_f32 v[122:123], v[122:123], v[126:127]\n"
    "s_waitcnt lgkmcnt(0)\n"
    "v_pk_add_f32 v[128:129], v[128:129], v[132:133]\n"
    "v_pk_add_f32 v[130:131], v[130:131], v[134:135]\n"
    "v_pk_add_f32 v[104:105], v[104:105], v[112:113]\n"
    "v_pk_add_f32 v[106:107], v[106:107], v[114:115]\n"
    "v_pk_add_f32 v[120:121], v[120:121], v[128:129]\n"
    "v_pk_add_f32 v[122:123], v[122:123], v[130:131]\n"
    "v_pk_add_f32 v[104:105], v[104:105], v[120:121]\n"
    "v_pk_add_f32 v[106:107], v[106:107], v[122:123]\n"
    "v_fma_f32 v104, v104, s89, v144\n"
    "v_fma_f32 v105, v105, s89, v145\n"
    "v_fma_f32 v106, v106, s89, v146\n"
    "v_fma_f32 v107, v107, s89, v147\n"
    "s_mov_b32 s89, 0x3f888e99\n"
    "v_cvt_pk_bf16_f32 v104, v104, v105\n"
    "v_cvt_pk_bf16_f32 v105, v106, v107\n"
    "buffer_store_short v104, v139, s[52:55], s88 offen sc1\n"
    "buffer_store_short_d16_hi v104, v139, s[52:55], s88 offen offset:1152 sc1\n"
    "buffer_store_short v105, v139, s[52:55], s88 offen offset:2304 sc1\n"
    "buffer_store_short_d16_hi v105, v139, s[52:55], s88 offen offset:3456 sc1\n"
    ".Lmx_end:\n"
    :
    : "s"(ka), "s"(bid), "v"(tid), "s"(tab)
    : "s4", "s5", "s6", "s7", "s8", "s9", "s10", "s11", "s12", "s13", "s14", "s15", "s16", "s17", "v1", "v2", "v3", "v4", "v5", "v6", "v7", "v8", "v9", "v10", "v11", "v12", "v13", "v14", "v15", "v16", "v17", "v18", "v19", "v20", "v21", "v22", "v23", "v24", "v25", "v26", "v27", "v28", "v29", "v30", "v31", "v32", "v33", "v34", "v35", "v36", "v37", "v38", "v39", "v40", "v41", "v42", "v43", "v44", "v45", "v46", "v47", "v48", "v49", "v50", "v51", "v52", "v53", "v54", "v55", "v56", "v57", "v58", "v59", "v60", "v61", "v62", "v63", "v64", "v65", "v66", "v67", "v68", "v69", "v70", "v71", "v72", "v73", "v74", "v75", "v76", "v77", "v78", "v79", "v80", "v81", "v82", "v83", "v84", "v85", "v86", "v87", "v88", "v89", "v90", "v91", "v92", "v93", "v94", "v95", "v96", "v97", "v98", "v99", "v100", "v101", "v102", "v103", "v104", "v105", "v106", "v107", "v108", "v109", "v110", "v111", "v112", "v113", "v114", "v115", "v116", "v117", "v118", "v119", "v120", "v121", "v122", "v123", "v124", "v125", "v126", "v127", "v128", "v129", "v130", "v131", "v132", "v133", "v134", "v135", "v136", "v137", "v138", "v139", "v140", "v141", "v142", "v143", "v144", "v145", "v146", "v147", "v148", "v149", "v150", "v151", "v152", "v153", "v154", "v155", "v156", "v157", "v158", "v159", "v160", "v161", "v162", "v163", "v164", "v165", "v166", "v167", "v168", "v169", "v170", "v171", "v172", "v173", "v174", "v175", "v176", "v177", "v178", "v179", "v180", "v181", "v182", "v183", "v184", "v185", "v186", "v187", "v188", "v189", "v190", "v191", "v192", "v193", "v194", "v195", "v196", "v197", "v198", "v199", "v200", "v201", "v202", "v203", "v204", "v205", "v206", "v207", "v208", "v209", "v210", "v211", "v212", "v213", "v214", "v215", "v216", "v217", "v218", "v219", "v220", "v221", "v222", "v223", "v224", "v225", "v226", "v227", "v228", "v229", "v230", "v231", "v232", "v233", "v234", "v235", "v236", "v237", "v238", "v239", "v240", "v241", "v242", "v243", "v244", "v245", "v246", "v247", "v248", "v249", "v250", "v251", "v252", "v253", "v254", "v255", "s33", "s34", "s35", "s36", "s37", "s38", "s39", "s40", "s41", "s42", "s43", "s44", "s45", "s46", "s47", "s48", "s49", "s50", "s51", "s52", "s53", "s54", "s55", "s56", "s57", "s58", "s59", "s60", "s61", "s62", "s63", "s64", "s65", "s66", "s67", "s68", "s69", "s70", "s71", "s72", "s73", "s74", "s75", "s76", "s77", "s78", "s79", "s80", "s81", "s82", "s83", "s84", "s85", "s86", "s87", "s88", "s89", "s90", "s91", "s92", "s93", "s94", "s95", "s96", "s97", "s98", "s99", "vcc", "scc", "memory");
}
}

extern "C" void kernel_launch(void* const* d_in, const int* in_sizes, int n_in, void* d_out, int out_size,
                              void* d_ws, size_t ws_size, hipStream_t stream) {
  (void)in_sizes; (void)n_in; (void)out_size; (void)ws_size;
  MxArgs a;
  a.x = d_in[0]; a.w1 = d_in[1]; a.b1 = d_in[2]; a.w2 = d_in[3]; a.b2 = d_in[4]; a.out = d_out; a.ws = d_ws;
  (void)hipFuncSetAttribute((const void*)mixer_token_mlp, hipFuncAttributeMaxDynamicSharedMemorySize, 163840);
  mixer_token_mlp<<<256, 512, 163840, stream>>>(a);
}
```
